```python
import torch

def _softmax(x, axis=-1):
    return torch.softmax(x, dim=axis)
def _gelu(x):
    return torch.nn.functional.gelu(x, approximate="tanh")
def _silu(x):
    return torch.nn.functional.silu(x)
def _sigmoid(x):
    return torch.sigmoid(x)
def _layernorm(x, axis=-1, eps=1e-5):
    m = x.mean(dim=axis, keepdim=True)
    v = x.var(dim=axis, keepdim=True, unbiased=False)
    return (x - m) / torch.sqrt(v + eps)
def _scatter_add(tgt, idx, src, axis=0):
    return tgt.clone().index_add_(axis, idx, src)
def _segment_sum(src, seg, n):
    out = torch.zeros((n,) + src.shape[1:], dtype=src.dtype)
    return out.index_add_(0, seg, src)
def _pad(x, pw):
    flat = tuple(v for pair in reversed(pw) for v in pair)
    return torch.nn.functional.pad(x, flat)

kernel_name = 'concat_heads__ssm_conv_s4d__ssm_conv_s4d__prompt'

def setup_inputs():

    _w = (lambda a: (lambda t: t.to(torch.bfloat16) if t.is_floating_point() else t)(torch.as_tensor(a)))
    def _A_setup():
        Bsz, L, D, N = 1, 2048, 576, 128
        return {
            "u": _w((lambda *s: torch.randn(*s, dtype=torch.bfloat16) * 0.05)(Bsz, L, D)),
            "log_A_real": _w((lambda *s: torch.randn(*s, dtype=torch.bfloat16) * 6.4)(D, N) * 0.5),
            "A_imag": _w((lambda *s: torch.randn(*s, dtype=torch.bfloat16) * 0.05)(D, N) * 3.14),
            "C_real": _w((lambda *s: torch.randn(*s, dtype=torch.bfloat16) * 0.05)(D, N) / N ** 0.5),
            "C_imag": _w((lambda *s: torch.randn(*s, dtype=torch.bfloat16) * 0.05)(D, N) / N ** 0.5),
            "log_dt": _w((lambda *s: torch.randn(*s, dtype=torch.bfloat16) * 0.05)(D) * 0.5 - 4.0),
            "D_skip": _w((lambda *s: torch.randn(*s, dtype=torch.bfloat16) * 0.05)(D)),
        }
    _A_in = _A_setup()

    def _B_setup():
        Bsz, L, D, N = 1, 2048, 576, 128
        return {
            "u": _w((lambda *s: torch.randn(*s, dtype=torch.bfloat16) * 0.05)(Bsz, L, D)),
            "log_A_real": _w((lambda *s: torch.randn(*s, dtype=torch.bfloat16) * 6.4)(D, N) * 0.5),
            "A_imag": _w((lambda *s: torch.randn(*s, dtype=torch.bfloat16) * 0.05)(D, N) * 3.14),
            "C_real": _w((lambda *s: torch.randn(*s, dtype=torch.bfloat16) * 0.05)(D, N) / N ** 0.5),
            "C_imag": _w((lambda *s: torch.randn(*s, dtype=torch.bfloat16) * 0.05)(D, N) / N ** 0.5),
            "log_dt": _w((lambda *s: torch.randn(*s, dtype=torch.bfloat16) * 0.05)(D) * 0.5 - 4.0),
            "D_skip": _w((lambda *s: torch.randn(*s, dtype=torch.bfloat16) * 0.05)(D)),
        }
    _B_in = _B_setup()

    return {
        "A_u": _A_in["u"],
        "A_log_A_real": _A_in["log_A_real"],
        "A_A_imag": _A_in["A_imag"],
        "A_C_real": _A_in["C_real"],
        "A_C_imag": _A_in["C_imag"],
        "A_log_dt": _A_in["log_dt"],
        "A_D_skip": _A_in["D_skip"],
        "B_u": _B_in["u"],
        "B_log_A_real": _B_in["log_A_real"],
        "B_A_imag": _B_in["A_imag"],
        "B_C_real": _B_in["C_real"],
        "B_C_imag": _B_in["C_imag"],
        "B_log_dt": _B_in["log_dt"],
        "B_D_skip": _B_in["D_skip"],
    }

def reference(A_u, A_log_A_real, A_A_imag, A_C_real, A_C_imag, A_log_dt, A_D_skip, B_u, B_log_A_real, B_A_imag, B_C_real, B_C_imag, B_log_dt, B_D_skip):
    def _A_ref(u, log_A_real, A_imag, C_real, C_imag, log_dt, D_skip):
        L = u.shape[1]
        dt = torch.exp(log_dt)
        A = -torch.exp(log_A_real) + 1j * A_imag
        C = (C_real + 1j * C_imag) * dt[:, None]
        pos = (lambda x, dt: x.to(dt))(torch.arange(L), log_dt.dtype)
        P = torch.exp((A * dt[:, None])[:, :, None] * pos[None, None, :])
        K = 2.0 * torch.einsum("dn,dnl->dl", C, P).real
        L2 = 2 * L
        u_t = (lambda x, *d: x.permute(*d))(u, 0, 2, 1)
        Kf = (lambda x, *a, **k: torch.fft.rfft(x.float(), *a, **k))(K, L2)
        Uf = (lambda x, *a, **k: torch.fft.rfft(x.float(), *a, **k))(u_t, L2)
        y = (lambda x, *a, **k: torch.fft.irfft(x, *a, **k).to(torch.bfloat16))(Uf * Kf[None, :, :], L2)[:, :, :L]
        y = (lambda x, *d: x.permute(*d))(y, 0, 2, 1)
        return y + u * D_skip[None, None, :]

    def _B_ref(u, log_A_real, A_imag, C_real, C_imag, log_dt, D_skip):
        L = u.shape[1]
        dt = torch.exp(log_dt)
        A = -torch.exp(log_A_real) + 1j * A_imag
        C = (C_real + 1j * C_imag) * dt[:, None]
        pos = (lambda x, dt: x.to(dt))(torch.arange(L), log_dt.dtype)
        P = torch.exp((A * dt[:, None])[:, :, None] * pos[None, None, :])
        K = 2.0 * torch.einsum("dn,dnl->dl", C, P).real
        L2 = 2 * L
        u_t = (lambda x, *d: x.permute(*d))(u, 0, 2, 1)
        Kf = (lambda x, *a, **k: torch.fft.rfft(x.float(), *a, **k))(K, L2)
        Uf = (lambda x, *a, **k: torch.fft.rfft(x.float(), *a, **k))(u_t, L2)
        y = (lambda x, *a, **k: torch.fft.irfft(x, *a, **k).to(torch.bfloat16))(Uf * Kf[None, :, :], L2)[:, :, :L]
        y = (lambda x, *d: x.permute(*d))(y, 0, 2, 1)
        return y + u * D_skip[None, None, :]

    a = _A_ref(A_u, A_log_A_real, A_A_imag, A_C_real, A_C_imag, A_log_dt, A_D_skip)
    b = _B_ref(B_u, B_log_A_real, B_A_imag, B_C_real, B_C_imag, B_log_dt, B_D_skip)
    return torch.concatenate([a, b], axis=-1)
```

```cpp
#include <hip/hip_runtime.h>
#include <hip/hip_bf16.h>
#include <stdint.h>

#define LL 2048
#define DD 576
#define NN 128

__device__ __forceinline__ float bf2f(uint16_t v){ return __uint_as_float(((uint32_t)v)<<16); }
__device__ __forceinline__ uint16_t f2bf(float f){ uint32_t u=__float_as_uint(f); u = (u + 0x7FFF + ((u>>16)&1))>>16; return (uint16_t)u; }
__device__ __forceinline__ float rbf(float f){ return bf2f(f2bf(f)); }

__global__ void kgen(const uint16_t* lar, const uint16_t* aim, const uint16_t* cre, const uint16_t* cim, const uint16_t* ldt, float* K){
  int d = blockIdx.x;
  __shared__ float sa[NN], sb[NN], sc[NN], sd[NN];
  float dt = rbf(expf(bf2f(ldt[d])));
  if (threadIdx.x < NN){
    int n = threadIdx.x;
    float a = -rbf(expf(bf2f(lar[d*NN+n])));
    float b = bf2f(aim[d*NN+n]);
    sa[n] = a*dt; sb[n] = b*dt; sc[n] = bf2f(cre[d*NN+n])*dt; sd[n] = bf2f(cim[d*NN+n])*dt;
  }
  __syncthreads();
  for (int l = threadIdx.x; l < LL; l += blockDim.x){
    float p = rbf((float)l);
    float acc = 0.f;
    for (int n=0;n<NN;n++){
      float e = expf(sa[n]*p);
      float s,c; sincosf(sb[n]*p,&s,&c);
      acc += sc[n]*(e*c) - sd[n]*(e*s);
    }
    K[d*LL+l] = 2.f*acc;
  }
}

__global__ void conv(const uint16_t* u, const float* K, const uint16_t* dsk, uint16_t* out, int ooff){
  int d = blockIdx.x*64 + (threadIdx.x & 63);
  int l = blockIdx.y*4 + (threadIdx.x >> 6);
  float acc = 0.f;
  for (int j=0;j<=l;j++) acc += K[d*LL+j]*bf2f(u[(l-j)*DD+d]);
  float y = rbf(acc);
  float s = rbf(bf2f(u[l*DD+d])*bf2f(dsk[d]));
  out[l*(2*DD)+ooff+d] = f2bf(y+s);
}

extern "C" void kernel_launch(void* const* d_in, const int* in_sizes, int n_in,
                              void* d_out, int out_size, void* d_ws, size_t ws_size,
                              hipStream_t stream) {
  float* K = (float*)d_ws;
  for (int br=0;br<2;br++){
    const uint16_t* u = (const uint16_t*)d_in[br*7+0];
    const uint16_t* lar = (const uint16_t*)d_in[br*7+1];
    const uint16_t* aim = (const uint16_t*)d_in[br*7+2];
    const uint16_t* cre = (const uint16_t*)d_in[br*7+3];
    const uint16_t* cim = (const uint16_t*)d_in[br*7+4];
    const uint16_t* ldt = (const uint16_t*)d_in[br*7+5];
    const uint16_t* dsk = (const uint16_t*)d_in[br*7+6];
    float* Kb = K + br*DD*LL;
    kgen<<<DD,256,0,stream>>>(lar,aim,cre,cim,ldt,Kb);
    conv<<<dim3(DD/64,LL/4),256,0,stream>>>(u,Kb,dsk,(uint16_t*)d_out,br*DD);
  }
}
```

```cpp
#include <hip/hip_runtime.h>
#include <hip/hip_bf16.h>
#include <stdint.h>

typedef float f32x2 __attribute__((ext_vector_type(2)));
typedef float f32x4 __attribute__((ext_vector_type(4)));
typedef float f32x16 __attribute__((ext_vector_type(16)));
typedef __bf16 bf16x8 __attribute__((ext_vector_type(8)));
typedef __bf16 bf16x2 __attribute__((ext_vector_type(2)));
typedef uint32_t u32x4 __attribute__((ext_vector_type(4)));
typedef uint32_t u32x2 __attribute__((ext_vector_type(2)));
typedef short s16x4 __attribute__((ext_vector_type(4)));
typedef unsigned char u8;
#define LDS __attribute__((address_space(3)))

#define STAMP(i)

constexpr int SEQ = 2048, DCH = 576, NMODE = 128;
constexpr int UPAD = 240;
constexpr int USTRIDE = (UPAD + SEQ) * 2;
constexpr int UBYTES = 8 * USTRIDE;
constexpr int SHALF = 56064;
constexpr int YOFF = 16 * 1424;
constexpr int UOFF = 2 * SHALF;
constexpr int SMEM = UOFF + UBYTES;
constexpr int RSTRIDE = 8000;
constexpr int KD0 = 0, KD1 = 1216, K4O = 2336, K8O = 2848, YSO = 3200;

struct Params { const uint16_t* p[14]; uint16_t* out; };

__device__ __forceinline__ float bflo(uint32_t v) { return __uint_as_float(v << 16); }
__device__ __forceinline__ float bfhi(uint32_t v) { return __uint_as_float(v & 0xFFFF0000u); }
__device__ __forceinline__ uint32_t pk(float lo, float hi) {
  f32x2 v = {lo, hi};
  return __builtin_bit_cast(uint32_t, __builtin_convertvector(v, bf16x2));
}
__device__ __forceinline__ uint32_t pk2(f32x2 v) { return __builtin_bit_cast(uint32_t, __builtin_convertvector(v, bf16x2)); }
__device__ __forceinline__ float rbf(float x) { return bflo(pk(x, x)); }
__device__ __forceinline__ float exp_acc(float x) {
  const float L = 1.44269502162933349609375f, Llo = 1.925963033500011e-8f;
  float t = x * L;
  float r = __builtin_fmaf(x, L, -t);
  r = __builtin_fmaf(x, Llo, r);
  float e = __builtin_amdgcn_exp2f(t);
  return __builtin_fmaf(e * r, 0.693147180559945f, e);
}
__device__ __forceinline__ f32x2 cmul(f32x2 x, f32x2 w) {
  f32x2 t = x * w.xx;
  f32x2 s = {-w.y, w.y};
  return __builtin_elementwise_fma(x.yx, s, t);
}
__device__ __forceinline__ f32x2 cpow_direct(float al, float be, float e) {
  float m = __builtin_amdgcn_exp2f(al * e);
  float ph = __builtin_amdgcn_fractf(be * e);
  f32x2 r = {m * __builtin_amdgcn_cosf(ph), m * __builtin_amdgcn_sinf(ph)};
  return r;
}
template <int OFF> __device__ __forceinline__ void wr_tid(uint32_t v) {
  asm volatile("ds_write_addtid_b32 %0 offset:%1" ::"v"(v), "i"(OFF) : "memory");
}
__device__ __forceinline__ f32x4 mma(u32x4 a, u32x4 b, f32x4 c) {
  return __builtin_amdgcn_mfma_f32_16x16x32_bf16(__builtin_bit_cast(bf16x8, a), __builtin_bit_cast(bf16x8, b), c, 0, 0, 0);
}
__device__ __forceinline__ f32x16 mma32(u32x4 a, u32x4 b, f32x16 c) {
  return __builtin_amdgcn_mfma_f32_32x32x16_bf16(__builtin_bit_cast(bf16x8, a), __builtin_bit_cast(bf16x8, b), c, 0, 0, 0);
}
__device__ __forceinline__ u32x4 ld128(LDS u8* p) { return *(LDS u32x4*)p; }
__device__ __forceinline__ void bar() {
  asm volatile("s_waitcnt lgkmcnt(0)\n\ts_barrier" ::: "memory");
}

template <int N> struct IC { static constexpr int v = N; };
template <int B, int E, class F> __device__ __forceinline__ void sfor(F&& f) {
  if constexpr (B < E) { f(IC<B>{}); sfor<B + 1, E>(f); }
}

__global__ __launch_bounds__(512, 2) void s4d_fused_kernel(Params P
) {
  __shared__ __attribute__((aligned(256))) u8 smem_[SMEM];
  LDS u8* smem = (LDS u8*)smem_;
  const int tid = threadIdx.x, lane = tid & 63;
  const int w = __builtin_amdgcn_readfirstlane(tid >> 6);
  const int xcd = blockIdx.x & 7, wl = blockIdx.x >> 3;
  const int br = xcd >> 2;
  const int ch0 = (xcd & 3) * 144 + wl * 8;
  const int d = ch0 + w;
  STAMP(0);
  const uint16_t* pu  = br ? P.p[7]  : P.p[0];
  const uint16_t* pla = br ? P.p[8]  : P.p[1];
  const uint16_t* pai = br ? P.p[9]  : P.p[2];
  const uint16_t* pcr = br ? P.p[10] : P.p[3];
  const uint16_t* pci = br ? P.p[11] : P.p[4];
  const uint16_t* pdt = br ? P.p[12] : P.p[5];
  const uint16_t* pds = br ? P.p[13] : P.p[6];

  const uint32_t g_la = ((const uint32_t*)pla)[d * 64 + lane];
  const uint32_t g_ai = ((const uint32_t*)pai)[d * 64 + lane];
  const uint32_t g_cr = ((const uint32_t*)pcr)[d * 64 + lane];
  const uint32_t g_ci = ((const uint32_t*)pci)[d * 64 + lane];
  const uint32_t g_dt = pdt[d];
  const uint32_t g_ds = pds[d];
  uint32_t pf;
  {
    int idx = min(wl * 342 + min(tid, 341), 6143);
    int row = idx / 3, ln = idx - 3 * row;
    pf = *(const uint32_t*)((const u8*)pu + row * (DCH * 2) + (2 * (xcd & 3) + ln) * 128);
  }

  LDS u8* reg = smem + w * RSTRIDE;
  LDS u8* stg = smem + (w >> 2) * SHALF + (w & 3) * 256;
  LDS u8* ub = smem + UOFF + w * USTRIDE;
  const int c16 = lane & 15, g4 = lane >> 4;
  const int r32 = lane & 31, h2 = lane >> 5;

  if (lane < 60) { u32x2 z = {0, 0}; *(LDS u32x2*)(ub + 8 * lane) = z; }

  int xunit;
  if (r32 < 8) xunit = 65 * r32;
  else if (r32 < 16) xunit = 65 * (r32 - 7);
  else if (r32 < 24) { int t = (int)((0xCBCBAA999999DBBAull >> (8 * (r32 - 16))) & 0xFF); xunit = 64 * (t >> 4) + (t & 15); }
  else if (r32 < 28) xunit = 908 + 129 * (r32 - 24);
  else xunit = 972 + 129 * (r32 - 28);
  LDS u8* xrd = stg + 16 * xunit + 16 * h2;
  LDS u8* yrd = stg + YOFF + 1040 * r32 + 16 * h2;
  int zoff;
  if (lane < 7) zoff = KD0 + 8 * lane;
  else if (lane < 10) zoff = KD0 + 1096 + 8 * (lane - 7);
  else if (lane < 17) zoff = KD1 + 8 * (lane - 10);
  else if (lane < 21) zoff = KD1 + 1088 + 8 * (lane - 17);
  else if (lane < 37) zoff = K4O + 8 * (lane - 21);
  else if (lane < 53) zoff = K4O + 384 + 8 * (lane - 37);
  else zoff = K8O + 256 + 8 * min(lane - 53, 7);

  auto band = [&](int centre, int half) {
    u32x4 r;
    #pragma unroll
    for (int i = 0; i < 4; i++) {
      int k = 8 * g4 + 2 * i - centre - c16;
      uint32_t lo = (k >= -half && k <= half) ? 0x3F80u : 0u;
      uint32_t hi = (k + 1 >= -half && k + 1 <= half) ? 0x3F800000u : 0u;
      r[i] = lo | hi;
    }
    return r;
  };
  const u32x4 b4e = band(8, 2), b4o = band(12, 1), b8e = band(8, 4), b8o = band(8, 3);
  u32x4 cmask;
  #pragma unroll
  for (int i = 0; i < 4; i++) {
    int k = 8 * g4 + 2 * i - c16;
    cmask[i] = ((k == 12 || k == 13) ? 0xFFFFu : 0u) | ((k == 11 || k == 12) ? 0xFFFF0000u : 0u);
  }
  asm volatile("s_mov_b32 m0, %0\n\ts_nop 0" ::"s"((uint32_t)(uintptr_t)stg) : "memory");

  const float dt = rbf(exp_acc(bflo(g_dt)));
  const float dskip = bflo(g_ds);
  STAMP(1);

  u32x4 ur[4];
  {
    const u8* up = (const u8*)pu + (size_t)(256 * w + 4 * lane) * (DCH * 2) + ch0 * 2;
    #pragma unroll
    for (int i = 0; i < 4; i++) ur[i] = *(const u32x4*)(up + i * (DCH * 2));
  }
  asm volatile("" ::"v"(pf));

  f32x16 kacc;
  #pragma unroll
  for (int i = 0; i < 16; i++) kacc[i] = 0.f;
  #pragma unroll
  for (int rd = 0; rd < 2; rd++) {
    const float la = rd ? bfhi(g_la) : bflo(g_la);
    const float ai = rd ? bfhi(g_ai) : bflo(g_ai);
    const float cr = rd ? bfhi(g_cr) : bflo(g_cr);
    const float ci = rd ? bfhi(g_ci) : bflo(g_ci);
    const float a = -rbf(exp_acc(la)) * dt, b = ai * dt;
    const float al = a * 1.44269504088896341f, be = b * 0.159154943091895336f;
    const f32x2 w1 = cpow_direct(al, be, 1.f);
    f32x2 y8 = cpow_direct(al, be, 8.f); y8.y = -y8.y;
    const f32x2 y16 = cmul(y8, y8);
    f32x2 x = {2.f * (cr * dt), 2.f * (ci * dt)};
    f32x2 ye = {1.f, 0.f}, yo = y8;
    f32x2 xs[5];
    xs[0] = x;
    wr_tid<0>(pk2(x));
    sfor<0, 16>([&](auto J) {
      constexpr int j = J.v;
      wr_tid<YOFF + 1040 * (2 * j)>(j == 0 ? 0x00003F80u : pk2(ye));
      wr_tid<YOFF + 1040 * (2 * j + 1)>(pk2(yo));
      ye = cmul(ye, y16);
      if constexpr (j < 15) yo = cmul(yo, y16);
      if constexpr (j < 8) {
        x = cmul(x, w1);
        wr_tid<16 * 65 * (j + 1)>(pk2(x));
        if constexpr (j & 1) xs[(j + 1) / 2] = x;
      }
    });
    const f32x2 w256 = {ye.x, -ye.y};
    f32x2 x256 = cmul(xs[0], w256); wr_tid<16 * 714>(pk2(x256));
    f32x2 x258 = cmul(xs[1], w256); wr_tid<16 * 843>(pk2(x258));
    f32x2 x260 = cmul(xs[2], w256); wr_tid<16 * 585>(pk2(x260));
    f32x2 x262 = cmul(xs[3], w256); wr_tid<16 * 650>(pk2(x262));
    f32x2 x264 = cmul(xs[4], w256); wr_tid<16 * 779>(pk2(x264));
    f32x2 x516 = cmul(x260, w256);  wr_tid<16 * 908>(pk2(x516));
    f32x2 x520 = cmul(x264, w256);  wr_tid<16 * 1037>(pk2(x520));
    f32x2 x772 = cmul(x516, w256);  wr_tid<16 * 1166>(pk2(x772));
    f32x2 x776 = cmul(x520, w256);  wr_tid<16 * 1295>(pk2(x776));
    f32x2 xf = cmul(x776, w256);    wr_tid<16 * 972>(pk2(xf));
    xf = cmul(xf, w256);            wr_tid<16 * 1101>(pk2(xf));
    xf = cmul(xf, w256);            wr_tid<16 * 1230>(pk2(xf));
    xf = cmul(xf, w256);            wr_tid<16 * 1359>(pk2(xf));
    #pragma unroll
    for (int s = 0; s < 8; s++) kacc = mma32(ld128(xrd + 32 * s), ld128(yrd + 32 * s), kacc);
  }
  STAMP(2);

  #pragma unroll
  for (int dd = 0; dd < 4; dd++) {
    u32x2 lo, hi;
    lo.x = __builtin_amdgcn_perm(ur[1][dd], ur[0][dd], 0x05040100u);
    lo.y = __builtin_amdgcn_perm(ur[3][dd], ur[2][dd], 0x05040100u);
    hi.x = __builtin_amdgcn_perm(ur[1][dd], ur[0][dd], 0x07060302u);
    hi.y = __builtin_amdgcn_perm(ur[3][dd], ur[2][dd], 0x07060302u);
    *(LDS u32x2*)(smem + UOFF + (2 * dd) * USTRIDE + 2 * (UPAD + 256 * w + 4 * lane)) = lo;
    *(LDS u32x2*)(smem + UOFF + (2 * dd + 1) * USTRIDE + 2 * (UPAD + 256 * w + 4 * lane)) = hi;
  }

  STAMP(3);
  bar();
  STAMP(4);

  {
    u32x2 z = {0, 0};
    asm volatile("" ::: "memory");
    *(LDS u32x2*)(reg + zoff) = z;
    const int xo = 2 * (540 - 8 * r32 - 4 * h2);
    const float k0 = kacc[0] + ((lane == 0) ? dskip : 0.f);
    u32x2 v;
    v.x = pk(kacc[3], kacc[2]); v.y = pk(kacc[1], k0);            *(LDS u32x2*)(reg + KD1 + xo) = v;
    v.x = pk(kacc[7], kacc[6]); v.y = pk(kacc[5], kacc[4]);       *(LDS u32x2*)(reg + KD0 + xo) = v;
    v.x = pk(kacc[10], kacc[9]); v.y = pk(kacc[8], kacc[8]);      *(LDS u32x2*)(reg + KD1 + xo - 512) = v;
    v.x = pk(kacc[10], kacc[10]); v.y = pk(kacc[9], kacc[8]);     *(LDS u32x2*)(reg + KD0 + xo - 512) = v;
    if (lane == 63) {
      v.x = pk(0.f, kacc[10]); v.y = pk(kacc[10], kacc[10]);      *(LDS u32x2*)(reg + KD1 + 56) = v;
      v.x = 0; v.y = pk(kacc[10], kacc[10]);                      *(LDS u32x2*)(reg + KD0 + 56) = v;
    }
    if (lane == 0) { v.x = pk(k0, 0.f); v.y = 0;                  *(LDS u32x2*)(reg + KD0 + 1088) = v; }
    if (h2 == 0) {
      *(LDS uint32_t*)(reg + K4O + 2 * (190 - 2 * r32)) = pk(kacc[13], kacc[12]);
      *(LDS uint32_t*)(reg + K4O + 2 * (126 - 2 * r32)) = pk(kacc[15], kacc[14]);
    } else {
      uint32_t p0 = pk(kacc[12], kacc[13]), p1 = pk(kacc[14], kacc[15]);
      *(LDS uint16_t*)(reg + K8O + 2 * (127 - r32)) = (uint16_t)p0;
      *(LDS uint16_t*)(reg + K8O + 2 * (95 - r32)) = (uint16_t)(p0 >> 16);
      *(LDS uint16_t*)(reg + K8O + 2 * (63 - r32)) = (uint16_t)p1;
      *(LDS uint16_t*)(reg + K8O + 2 * (31 - r32)) = (uint16_t)(p1 >> 16);
    }
  }
  u32x4 kc;
  {
    uint32_t kv = __builtin_amdgcn_readlane(pk(kacc[15], kacc[15]), 31);
    #pragma unroll
    for (int i = 0; i < 4; i++) kc[i] = kv & cmask[i];
  }

  u32x4 kd[17], k4[6], k8[4];
  {
    LDS u8* kb = reg + ((c16 & 1) ? KD1 : KD0) + 4 * (264 - ((c16 + 1) >> 1) + 4 * g4);
    #pragma unroll
    for (int q = 0; q < 17; q++) {
      LDS uint32_t* p = (LDS uint32_t*)(kb - 64 * q);
      kd[q][0] = p[0]; kd[q][1] = p[1]; kd[q][2] = p[2]; kd[q][3] = p[3];
    }
    LDS u8* k4b = reg + K4O + 2 * (316 - 4 * c16 + 8 * g4);
    #pragma unroll
    for (int s = 3; s <= 8; s++) {
      u32x2 lo = *(LDS u32x2*)(k4b - 64 * s), hi = *(LDS u32x2*)(k4b - 64 * s + 8);
      k4[s - 3][0] = lo.x; k4[s - 3][1] = lo.y; k4[s - 3][2] = hi.x; k4[s - 3][3] = hi.y;
    }
    LDS u8* k8b = reg + K8O + 2 * (254 - 2 * c16 + 8 * g4);
    #pragma unroll
    for (int s = 4; s <= 7; s++) {
      LDS uint32_t* p = (LDS uint32_t*)(k8b - 64 * s);
      k8[s - 4][0] = p[0]; k8[s - 4][1] = p[1]; k8[s - 4][2] = p[2]; k8[s - 4][3] = p[3];
    }
  }
  STAMP(8);

  LDS u8* uf = ub + 2 * (UPAD - 16 + 16 * c16 + 8 * g4);
  LDS u8* u4 = ub + 2 * (UPAD - 24 + 8 * c16 + 8 * g4);
  LDS u8* ysw = reg + YSO + 144 * g4 + 8 * c16;
  f32x4 acc[8];
  #pragma unroll
  for (int i = 0; i < 8; i++) acc[i] = f32x4{0.f, 0.f, 0.f, 0.f};
  u32x4 v8[4];
  const f32x4 zero4 = {0.f, 0.f, 0.f, 0.f};
  LDS u8* ytr = smem + ((lane >> 2) & 3) * RSTRIDE + YSO + 144 * (lane & 3) + 8 * g4;
  uint16_t* outp = P.out + (size_t)(16 * g4 + c16) * (2 * DCH) + br * DCH + ch0;

  sfor<0, 8>([&](auto SS) {
    constexpr int S = SS.v;
    sfor<0, 8>([&](auto RR) {
      constexpr int r = 7 - RR.v;
      u32x4 f = ld128(uf + 64 * (8 * S - r));
      acc[S] = mma(kd[r], f, acc[S]);
      if constexpr (S + 1 < 8) acc[S + 1] = mma(kd[8 + r], f, acc[S + 1]);
      if constexpr (r == 0 && S + 2 < 8) acc[S + 2] = mma(kd[16], f, acc[S + 2]);
    });
    if constexpr (S >= 2) {
      sfor<0, 2>([&](auto PP) {
        constexpr int phi = 2 * S - 4 + PP.v;
        u32x4 f = ld128(u4 + 256 * phi);
        f32x4 e = mma(f, b4e, zero4), o = mma(f, b4o, zero4);
        u32x4 v = {pk(e[0], o[0]), pk(e[1], o[1]), pk(e[2], o[2]), pk(e[3], o[3])};
        sfor<0, 3>([&](auto TT) {
          constexpr int T = S + TT.v;
          if constexpr (T < 8) acc[T] = mma(v, k4[2 * T - phi - 3], acc[T]);
        });
      });
    }
    if constexpr (S >= 4) {
      constexpr int phi = S - 4;
      u32x4 fe = ld128(uf + 512 * phi - 16), fo = ld128(uf + 512 * phi);
      f32x4 e = mma(fe, b8e, zero4), o = mma(fo, b8o, zero4);
      v8[phi] = u32x4{pk(e[0], o[0]), pk(e[1], o[1]), pk(e[2], o[2]), pk(e[3], o[3])};
      sfor<0, phi + 1>([&](auto FF) {
        constexpr int f = FF.v;
        acc[S] = mma(v8[f], k8[S - f - 4], acc[S]);
      });
      acc[S] = mma(kc, fo, acc[S]);
    }
    u32x2 y = {pk(acc[S][0], acc[S][1]), pk(acc[S][2], acc[S][3])};
    *(LDS u32x2*)(ysw + 576 * S) = y;
    bar();
    if (w == S) {
      #pragma unroll
      for (int it = 0; it < 4; it++) {
        s16x4 lo = __builtin_amdgcn_ds_read_tr16_b64_v4i16((LDS s16x4*)(ytr + 576 * S + 32 * it));
        s16x4 hi = __builtin_amdgcn_ds_read_tr16_b64_v4i16((LDS s16x4*)(ytr + 576 * S + 32 * it + 4 * RSTRIDE));
        u32x2 l2 = __builtin_bit_cast(u32x2, lo), h2v = __builtin_bit_cast(u32x2, hi);
        u32x4 o4 = {l2.x, l2.y, h2v.x, h2v.y};
        *(u32x4*)(outp + (size_t)(256 * S + 64 * it) * (2 * DCH)) = o4;
      }
    }
    if constexpr (S == 0) { STAMP(5); }
    if constexpr (S == 3) { STAMP(6); }
  });
  STAMP(7);
}

extern "C" void kernel_launch(void* const* d_in, const int* in_sizes, int n_in,
                              void* d_out, int out_size, void* d_ws, size_t ws_size,
                              hipStream_t stream) {
  Params P;
  for (int i = 0; i < 14; i++) P.p[i] = (const uint16_t*)d_in[i];
  P.out = (uint16_t*)d_out;
  s4d_fused_kernel<<<144, 512, 0, stream>>>(P);
}
```

```cpp
#include <hip/hip_runtime.h>
#include <hip/hip_bf16.h>
#include <stdint.h>

typedef float f32x2 __attribute__((ext_vector_type(2)));
typedef float f32x4 __attribute__((ext_vector_type(4)));
typedef float f32x16 __attribute__((ext_vector_type(16)));
typedef __bf16 bf16x8 __attribute__((ext_vector_type(8)));
typedef __bf16 bf16x2 __attribute__((ext_vector_type(2)));
typedef uint32_t u32x4 __attribute__((ext_vector_type(4)));
typedef uint32_t u32x2 __attribute__((ext_vector_type(2)));
typedef short s16x4 __attribute__((ext_vector_type(4)));
typedef unsigned char u8;
#define LDS __attribute__((address_space(3)))

#define STAMP(i)
#define RSTAMP(i)

constexpr int SEQ = 2048, DCH = 576, NMODE = 128;
constexpr int UPAD = 240;
constexpr int USTRIDE = (UPAD + SEQ) * 2;
constexpr int UBYTES = 8 * USTRIDE;
constexpr int SHALF = 56064;
constexpr int YOFF = 16 * 1424;
constexpr int UOFF = 2 * SHALF;
constexpr int SMEM = UOFF + UBYTES;
constexpr int RSTRIDE = 8000;
constexpr int KD0 = 0, KD1 = 1216, K4O = 2336, K8O = 2848, YSO = 3200;

struct Params { const uint16_t* p[14]; uint16_t* out; };

__device__ __forceinline__ float bflo(uint32_t v) { return __uint_as_float(v << 16); }
__device__ __forceinline__ float bfhi(uint32_t v) { return __uint_as_float(v & 0xFFFF0000u); }
__device__ __forceinline__ uint32_t pk(float lo, float hi) {
  f32x2 v = {lo, hi};
  return __builtin_bit_cast(uint32_t, __builtin_convertvector(v, bf16x2));
}
__device__ __forceinline__ uint32_t pk2(f32x2 v) { return __builtin_bit_cast(uint32_t, __builtin_convertvector(v, bf16x2)); }
__device__ __forceinline__ float rbf(float x) { return bflo(pk(x, x)); }
__device__ __forceinline__ float exp_acc(float x) {
  const float L = 1.44269502162933349609375f, Llo = 1.925963033500011e-8f;
  float t = x * L;
  float r = __builtin_fmaf(x, L, -t);
  r = __builtin_fmaf(x, Llo, r);
  float e = __builtin_amdgcn_exp2f(t);
  return __builtin_fmaf(e * r, 0.693147180559945f, e);
}
__device__ __forceinline__ f32x2 cmul(f32x2 x, f32x2 w) {
  f32x2 t = x * w.xx;
  f32x2 s = {-w.y, w.y};
  return __builtin_elementwise_fma(x.yx, s, t);
}
__device__ __forceinline__ f32x2 cpow_direct(float al, float be, float e) {
  float m = __builtin_amdgcn_exp2f(al * e);
  float ph = __builtin_amdgcn_fractf(be * e);
  f32x2 r = {m * __builtin_amdgcn_cosf(ph), m * __builtin_amdgcn_sinf(ph)};
  return r;
}
template <int OFF> __device__ __forceinline__ void wr_tid(uint32_t v) {
  asm volatile("ds_write_addtid_b32 %0 offset:%1" ::"v"(v), "i"(OFF) : "memory");
}
__device__ __forceinline__ f32x4 mma(u32x4 a, u32x4 b, f32x4 c) {
  return __builtin_amdgcn_mfma_f32_16x16x32_bf16(__builtin_bit_cast(bf16x8, a), __builtin_bit_cast(bf16x8, b), c, 0, 0, 0);
}
__device__ __forceinline__ f32x16 mma32(u32x4 a, u32x4 b, f32x16 c) {
  return __builtin_amdgcn_mfma_f32_32x32x16_bf16(__builtin_bit_cast(bf16x8, a), __builtin_bit_cast(bf16x8, b), c, 0, 0, 0);
}
__device__ __forceinline__ u32x4 ld128(LDS u8* p) { return *(LDS u32x4*)p; }
__device__ __forceinline__ void bar() {
  asm volatile("s_waitcnt lgkmcnt(0)\n\ts_barrier" ::: "memory");
}

template <int N> struct IC { static constexpr int v = N; };
template <int B, int E, class F> __device__ __forceinline__ void sfor(F&& f) {
  if constexpr (B < E) { f(IC<B>{}); sfor<B + 1, E>(f); }
}

__global__ __launch_bounds__(512, 2) void s4d_fused_kernel(Params P
) {
  __shared__ __attribute__((aligned(256))) u8 smem_[SMEM];
  LDS u8* smem = (LDS u8*)smem_;
  const int tid = threadIdx.x, lane = tid & 63;
  const int w = __builtin_amdgcn_readfirstlane(tid >> 6);
  const int xcd = blockIdx.x & 7, wl = blockIdx.x >> 3;
  const int br = xcd >> 2;
  const int ch0 = (xcd & 3) * 144 + wl * 8;
  const int d = ch0 + w;
  STAMP(0);
  RSTAMP(14);
  const uint16_t* pu  = br ? P.p[7]  : P.p[0];
  const uint16_t* pla = br ? P.p[8]  : P.p[1];
  const uint16_t* pai = br ? P.p[9]  : P.p[2];
  const uint16_t* pcr = br ? P.p[10] : P.p[3];
  const uint16_t* pci = br ? P.p[11] : P.p[4];
  const uint16_t* pdt = br ? P.p[12] : P.p[5];
  const uint16_t* pds = br ? P.p[13] : P.p[6];

  const uint32_t g_la = ((const uint32_t*)pla)[d * 64 + lane];
  const uint32_t g_ai = ((const uint32_t*)pai)[d * 64 + lane];
  const uint32_t g_cr = ((const uint32_t*)pcr)[d * 64 + lane];
  const uint32_t g_ci = ((const uint32_t*)pci)[d * 64 + lane];
  const uint32_t g_dt = pdt[d];
  const uint32_t g_ds = pds[d];
  uint32_t pf;
  {
    int idx = min(wl * 342 + min(tid, 341), 6143);
    int row = idx / 3, ln = idx - 3 * row;
    pf = *(const uint32_t*)((const u8*)pu + row * (DCH * 2) + (2 * (xcd & 3) + ln) * 128);
  }

  LDS u8* reg = smem + w * RSTRIDE;
  LDS u8* stg = smem + (w >> 2) * SHALF + (w & 3) * 256;
  LDS u8* ub = smem + UOFF + w * USTRIDE;
  const int c16 = lane & 15, g4 = lane >> 4;
  const int r32 = lane & 31, h2 = lane >> 5;

  if (lane < 60) { u32x2 z = {0, 0}; *(LDS u32x2*)(ub + 8 * lane) = z; }

  int xunit;
  if (r32 < 8) xunit = 65 * r32;
  else if (r32 < 16) xunit = 65 * (r32 - 7);
  else if (r32 < 24) { int t = (int)((0xCBCBAA999999DBBAull >> (8 * (r32 - 16))) & 0xFF); xunit = 64 * (t >> 4) + (t & 15); }
  else if (r32 < 28) xunit = 908 + 129 * (r32 - 24);
  else xunit = 972 + 129 * (r32 - 28);
  LDS u8* xrd = stg + 16 * xunit + 16 * h2;
  LDS u8* yrd = stg + YOFF + 1040 * r32 + 16 * h2;
  int zoff;
  if (lane < 7) zoff = KD0 + 8 * lane;
  else if (lane < 10) zoff = KD0 + 1096 + 8 * (lane - 7);
  else if (lane < 17) zoff = KD1 + 8 * (lane - 10);
  else if (lane < 21) zoff = KD1 + 1088 + 8 * (lane - 17);
  else if (lane < 37) zoff = K4O + 8 * (lane - 21);
  else if (lane < 53) zoff = K4O + 384 + 8 * (lane - 37);
  else zoff = K8O + 256 + 8 * min(lane - 53, 7);

  auto band = [&](int centre, int half) {
    u32x4 r;
    #pragma unroll
    for (int i = 0; i < 4; i++) {
      int k = 8 * g4 + 2 * i - centre - c16;
      uint32_t lo = (k >= -half && k <= half) ? 0x3F80u : 0u;
      uint32_t hi = (k + 1 >= -half && k + 1 <= half) ? 0x3F800000u : 0u;
      r[i] = lo | hi;
    }
    return r;
  };
  const u32x4 b4e = band(8, 2), b4o = band(12, 1), b8e = band(8, 4), b8o = band(8, 3);
  u32x4 cmask;
  #pragma unroll
  for (int i = 0; i < 4; i++) {
    int k = 8 * g4 + 2 * i - c16;
    cmask[i] = ((k == 12 || k == 13) ? 0xFFFFu : 0u) | ((k == 11 || k == 12) ? 0xFFFF0000u : 0u);
  }
  asm volatile("s_mov_b32 m0, %0\n\ts_nop 0" ::"s"((uint32_t)(uintptr_t)stg) : "memory");

  const float dt = rbf(exp_acc(bflo(g_dt)));
  const float dskip = bflo(g_ds);
  STAMP(1);

  u32x4 ur[4];
  {
    const u8* up = (const u8*)pu + (size_t)(256 * w + 4 * lane) * (DCH * 2) + ch0 * 2;
    #pragma unroll
    for (int i = 0; i < 4; i++) ur[i] = *(const u32x4*)(up + i * (DCH * 2));
  }
  asm volatile("" ::"v"(pf));

  f32x16 kacc;
  #pragma unroll
  for (int i = 0; i < 16; i++) kacc[i] = 0.f;
  #pragma unroll
  for (int rd = 0; rd < 2; rd++) {
    const float la = rd ? bfhi(g_la) : bflo(g_la);
    const float ai = rd ? bfhi(g_ai) : bflo(g_ai);
    const float cr = rd ? bfhi(g_cr) : bflo(g_cr);
    const float ci = rd ? bfhi(g_ci) : bflo(g_ci);
    const float a = -rbf(exp_acc(la)) * dt, b = ai * dt;
    const float al = a * 1.44269504088896341f, be = b * 0.159154943091895336f;
    const f32x2 w1 = cpow_direct(al, be, 1.f);
    f32x2 y8 = cpow_direct(al, be, 8.f); y8.y = -y8.y;
    const f32x2 y16 = cmul(y8, y8);
    f32x2 x = {2.f * (cr * dt), 2.f * (ci * dt)};
    f32x2 ye = {1.f, 0.f}, yo = y8;
    f32x2 xs[5];
    xs[0] = x;
    wr_tid<0>(pk2(x));
    sfor<0, 16>([&](auto J) {
      constexpr int j = J.v;
      wr_tid<YOFF + 1040 * (2 * j)>(j == 0 ? 0x00003F80u : pk2(ye));
      wr_tid<YOFF + 1040 * (2 * j + 1)>(pk2(yo));
      ye = cmul(ye, y16);
      if constexpr (j < 15) yo = cmul(yo, y16);
      if constexpr (j < 8) {
        x = cmul(x, w1);
        wr_tid<16 * 65 * (j + 1)>(pk2(x));
        if constexpr (j & 1) xs[(j + 1) / 2] = x;
      }
    });
    const f32x2 w256 = {ye.x, -ye.y};
    f32x2 x256 = cmul(xs[0], w256); wr_tid<16 * 714>(pk2(x256));
    f32x2 x258 = cmul(xs[1], w256); wr_tid<16 * 843>(pk2(x258));
    f32x2 x260 = cmul(xs[2], w256); wr_tid<16 * 585>(pk2(x260));
    f32x2 x262 = cmul(xs[3], w256); wr_tid<16 * 650>(pk2(x262));
    f32x2 x264 = cmul(xs[4], w256); wr_tid<16 * 779>(pk2(x264));
    f32x2 x516 = cmul(x260, w256);  wr_tid<16 * 908>(pk2(x516));
    f32x2 x520 = cmul(x264, w256);  wr_tid<16 * 1037>(pk2(x520));
    f32x2 x772 = cmul(x516, w256);  wr_tid<16 * 1166>(pk2(x772));
    f32x2 x776 = cmul(x520, w256);  wr_tid<16 * 1295>(pk2(x776));
    f32x2 xf = cmul(x776, w256);    wr_tid<16 * 972>(pk2(xf));
    xf = cmul(xf, w256);            wr_tid<16 * 1101>(pk2(xf));
    xf = cmul(xf, w256);            wr_tid<16 * 1230>(pk2(xf));
    xf = cmul(xf, w256);            wr_tid<16 * 1359>(pk2(xf));
    #pragma unroll
    for (int s = 0; s < 8; s++) kacc = mma32(ld128(xrd + 32 * s), ld128(yrd + 32 * s), kacc);
  }
  STAMP(2);

  #pragma unroll
  for (int dd = 0; dd < 4; dd++) {
    u32x2 lo, hi;
    lo.x = __builtin_amdgcn_perm(ur[1][dd], ur[0][dd], 0x05040100u);
    lo.y = __builtin_amdgcn_perm(ur[3][dd], ur[2][dd], 0x05040100u);
    hi.x = __builtin_amdgcn_perm(ur[1][dd], ur[0][dd], 0x07060302u);
    hi.y = __builtin_amdgcn_perm(ur[3][dd], ur[2][dd], 0x07060302u);
    *(LDS u32x2*)(smem + UOFF + (2 * dd) * USTRIDE + 2 * (UPAD + 256 * w + 4 * lane)) = lo;
    *(LDS u32x2*)(smem + UOFF + (2 * dd + 1) * USTRIDE + 2 * (UPAD + 256 * w + 4 * lane)) = hi;
  }

  STAMP(3);
  bar();
  STAMP(4);

  {
    u32x2 z = {0, 0};
    asm volatile("" ::: "memory");
    *(LDS u32x2*)(reg + zoff) = z;
    const int xo = 2 * (540 - 8 * r32 - 4 * h2);
    const float k0 = kacc[0] + ((lane == 0) ? dskip : 0.f);
    u32x2 v;
    v.x = pk(kacc[3], kacc[2]); v.y = pk(kacc[1], k0);            *(LDS u32x2*)(reg + KD1 + xo) = v;
    v.x = pk(kacc[7], kacc[6]); v.y = pk(kacc[5], kacc[4]);       *(LDS u32x2*)(reg + KD0 + xo) = v;
    v.x = pk(kacc[10], kacc[9]); v.y = pk(kacc[8], kacc[8]);      *(LDS u32x2*)(reg + KD1 + xo - 512) = v;
    v.x = pk(kacc[10], kacc[10]); v.y = pk(kacc[9], kacc[8]);     *(LDS u32x2*)(reg + KD0 + xo - 512) = v;
    if (lane == 63) {
      v.x = pk(0.f, kacc[10]); v.y = pk(kacc[10], kacc[10]);      *(LDS u32x2*)(reg + KD1 + 56) = v;
      v.x = 0; v.y = pk(kacc[10], kacc[10]);                      *(LDS u32x2*)(reg + KD0 + 56) = v;
    }
    if (lane == 0) { v.x = pk(k0, 0.f); v.y = 0;                  *(LDS u32x2*)(reg + KD0 + 1088) = v; }
    if (h2 == 0) {
      *(LDS uint32_t*)(reg + K4O + 2 * (190 - 2 * r32)) = pk(kacc[13], kacc[12]);
      *(LDS uint32_t*)(reg + K4O + 2 * (126 - 2 * r32)) = pk(kacc[15], kacc[14]);
    } else {
      uint32_t p0 = pk(kacc[12], kacc[13]), p1 = pk(kacc[14], kacc[15]);
      *(LDS uint16_t*)(reg + K8O + 2 * (127 - r32)) = (uint16_t)p0;
      *(LDS uint16_t*)(reg + K8O + 2 * (95 - r32)) = (uint16_t)(p0 >> 16);
      *(LDS uint16_t*)(reg + K8O + 2 * (63 - r32)) = (uint16_t)p1;
      *(LDS uint16_t*)(reg + K8O + 2 * (31 - r32)) = (uint16_t)(p1 >> 16);
    }
  }
  u32x4 kc;
  {
    uint32_t kv = __builtin_amdgcn_readlane(pk(kacc[15], kacc[15]), 31);
    #pragma unroll
    for (int i = 0; i < 4; i++) kc[i] = kv & cmask[i];
  }

  u32x4 kd[17], k4[6], k8[4];
  {
    LDS u8* kb = reg + ((c16 & 1) ? KD1 : KD0) + 4 * (264 - ((c16 + 1) >> 1) + 4 * g4);
    #pragma unroll
    for (int q = 0; q < 17; q++) {
      LDS uint32_t* p = (LDS uint32_t*)(kb - 64 * q);
      kd[q][0] = p[0]; kd[q][1] = p[1]; kd[q][2] = p[2]; kd[q][3] = p[3];
    }
    LDS u8* k4b = reg + K4O + 2 * (316 - 4 * c16 + 8 * g4);
    #pragma unroll
    for (int s = 3; s <= 8; s++) {
      u32x2 lo = *(LDS u32x2*)(k4b - 64 * s), hi = *(LDS u32x2*)(k4b - 64 * s + 8);
      k4[s - 3][0] = lo.x; k4[s - 3][1] = lo.y; k4[s - 3][2] = hi.x; k4[s - 3][3] = hi.y;
    }
    LDS u8* k8b = reg + K8O + 2 * (254 - 2 * c16 + 8 * g4);
    #pragma unroll
    for (int s = 4; s <= 7; s++) {
      LDS uint32_t* p = (LDS uint32_t*)(k8b - 64 * s);
      k8[s - 4][0] = p[0]; k8[s - 4][1] = p[1]; k8[s - 4][2] = p[2]; k8[s - 4][3] = p[3];
    }
  }
  STAMP(8);

  LDS u8* uf = ub + 2 * (UPAD - 16 + 16 * c16 + 8 * g4);
  LDS u8* u4 = ub + 2 * (UPAD - 24 + 8 * c16 + 8 * g4);
  LDS u8* ysw = reg + YSO + 144 * g4 + 8 * c16;
  f32x4 acc[8];
  #pragma unroll
  for (int i = 0; i < 8; i++) acc[i] = f32x4{0.f, 0.f, 0.f, 0.f};
  u32x4 v8[4];
  const f32x4 zero4 = {0.f, 0.f, 0.f, 0.f};
  LDS u8* ytr = smem + ((lane >> 2) & 3) * RSTRIDE + YSO + 144 * (lane & 3) + 8 * g4;
  uint16_t* outp = P.out + (size_t)(16 * g4 + c16) * (2 * DCH) + br * DCH + ch0;

  sfor<0, 8>([&](auto SS) {
    constexpr int S = SS.v;
    sfor<0, 8>([&](auto RR) {
      constexpr int r = 7 - RR.v;
      u32x4 f = ld128(uf + 64 * (8 * S - r));
      acc[S] = mma(kd[r], f, acc[S]);
      if constexpr (S + 1 < 8) acc[S + 1] = mma(kd[8 + r], f, acc[S + 1]);
      if constexpr (r == 0 && S + 2 < 8) acc[S + 2] = mma(kd[16], f, acc[S + 2]);
    });
    if constexpr (S >= 2) {
      sfor<0, 2>([&](auto PP) {
        constexpr int phi = 2 * S - 4 + PP.v;
        u32x4 f = ld128(u4 + 256 * phi);
        f32x4 e = mma(f, b4e, zero4), o = mma(f, b4o, zero4);
        u32x4 v = {pk(e[0], o[0]), pk(e[1], o[1]), pk(e[2], o[2]), pk(e[3], o[3])};
        sfor<0, 3>([&](auto TT) {
          constexpr int T = S + TT.v;
          if constexpr (T < 8) acc[T] = mma(v, k4[2 * T - phi - 3], acc[T]);
        });
      });
    }
    if constexpr (S >= 4) {
      constexpr int phi = S - 4;
      u32x4 fe = ld128(uf + 512 * phi - 16), fo = ld128(uf + 512 * phi);
      f32x4 e = mma(fe, b8e, zero4), o = mma(fo, b8o, zero4);
      v8[phi] = u32x4{pk(e[0], o[0]), pk(e[1], o[1]), pk(e[2], o[2]), pk(e[3], o[3])};
      sfor<0, phi + 1>([&](auto FF) {
        constexpr int f = FF.v;
        acc[S] = mma(v8[f], k8[S - f - 4], acc[S]);
      });
      acc[S] = mma(kc, fo, acc[S]);
    }
    u32x2 y = {pk(acc[S][0], acc[S][1]), pk(acc[S][2], acc[S][3])};
    *(LDS u32x2*)(ysw + 576 * S) = y;
    bar();
    if (w == S) {
      #pragma unroll
      for (int it = 0; it < 4; it++) {
        s16x4 lo = __builtin_amdgcn_ds_read_tr16_b64_v4i16((LDS s16x4*)(ytr + 576 * S + 32 * it));
        s16x4 hi = __builtin_amdgcn_ds_read_tr16_b64_v4i16((LDS s16x4*)(ytr + 576 * S + 32 * it + 4 * RSTRIDE));
        u32x2 l2 = __builtin_bit_cast(u32x2, lo), h2v = __builtin_bit_cast(u32x2, hi);
        u32x4 o4 = {l2.x, l2.y, h2v.x, h2v.y};
        *(u32x4*)(outp + (size_t)(256 * S + 64 * it) * (2 * DCH)) = o4;
      }
    }
    if constexpr (S == 0) { STAMP(5); }
    if constexpr (S == 3) { STAMP(6); }
  });
  STAMP(7);
  STAMP(9);
  RSTAMP(15);
}

extern "C" void kernel_launch(void* const* d_in, const int* in_sizes, int n_in,
                              void* d_out, int out_size, void* d_ws, size_t ws_size,
                              hipStream_t stream) {
  Params P;
  for (int i = 0; i < 14; i++) P.p[i] = (const uint16_t*)d_in[i];
  P.out = (uint16_t*)d_out;
  s4d_fused_kernel<<<144, 512, 0, stream>>>(P);
}
```

```cpp
#define PRN 1
#include <hip/hip_runtime.h>
#include <hip/hip_bf16.h>
#include <stdint.h>
#include <stdio.h>

typedef float f32x2 __attribute__((ext_vector_type(2)));
typedef float f32x4 __attribute__((ext_vector_type(4)));
typedef float f32x16 __attribute__((ext_vector_type(16)));
typedef __bf16 bf16x8 __attribute__((ext_vector_type(8)));
typedef __bf16 bf16x2 __attribute__((ext_vector_type(2)));
typedef uint32_t u32x4 __attribute__((ext_vector_type(4)));
typedef uint32_t u32x2 __attribute__((ext_vector_type(2)));
typedef short s16x4 __attribute__((ext_vector_type(4)));
typedef unsigned char u8;
#define LDS __attribute__((address_space(3)))

#define STAMP(i) do { stamps[i] = __builtin_amdgcn_s_memtime(); } while (0)
#define RSTAMP(i) do { stamps[i] = __builtin_amdgcn_s_memrealtime(); } while (0)

constexpr int SEQ = 2048, DCH = 576, NMODE = 128;
constexpr int UPAD = 240;
constexpr int USTRIDE = (UPAD + SEQ) * 2;
constexpr int UBYTES = 8 * USTRIDE;
constexpr int SHALF = 56064;
constexpr int YOFF = 16 * 1424;
constexpr int UOFF = 2 * SHALF;
constexpr int SMEM = UOFF + UBYTES;
constexpr int RSTRIDE = 8000;
constexpr int KD0 = 0, KD1 = 1216, K4O = 2336, K8O = 2848, YSO = 3200;

struct Params { const uint16_t* p[14]; uint16_t* out; };

__device__ __forceinline__ float bflo(uint32_t v) { return __uint_as_float(v << 16); }
__device__ __forceinline__ float bfhi(uint32_t v) { return __uint_as_float(v & 0xFFFF0000u); }
__device__ __forceinline__ uint32_t pk(float lo, float hi) {
  f32x2 v = {lo, hi};
  return __builtin_bit_cast(uint32_t, __builtin_convertvector(v, bf16x2));
}
__device__ __forceinline__ uint32_t pk2(f32x2 v) { return __builtin_bit_cast(uint32_t, __builtin_convertvector(v, bf16x2)); }
__device__ __forceinline__ float rbf(float x) { return bflo(pk(x, x)); }
__device__ __forceinline__ float exp_acc(float x) {
  const float L = 1.44269502162933349609375f, Llo = 1.925963033500011e-8f;
  float t = x * L;
  float r = __builtin_fmaf(x, L, -t);
  r = __builtin_fmaf(x, Llo, r);
  float e = __builtin_amdgcn_exp2f(t);
  return __builtin_fmaf(e * r, 0.693147180559945f, e);
}
__device__ __forceinline__ f32x2 cmul(f32x2 x, f32x2 w) {
  f32x2 t = x * w.xx;
  f32x2 s = {-w.y, w.y};
  return __builtin_elementwise_fma(x.yx, s, t);
}
__device__ __forceinline__ f32x2 cpow_direct(float al, float be, float e) {
  float m = __builtin_amdgcn_exp2f(al * e);
  float ph = __builtin_amdgcn_fractf(be * e);
  f32x2 r = {m * __builtin_amdgcn_cosf(ph), m * __builtin_amdgcn_sinf(ph)};
  return r;
}
template <int OFF> __device__ __forceinline__ void wr_tid(uint32_t v) {
  asm volatile("ds_write_addtid_b32 %0 offset:%1" ::"v"(v), "i"(OFF) : "memory");
}
__device__ __forceinline__ f32x4 mma(u32x4 a, u32x4 b, f32x4 c) {
  return __builtin_amdgcn_mfma_f32_16x16x32_bf16(__builtin_bit_cast(bf16x8, a), __builtin_bit_cast(bf16x8, b), c, 0, 0, 0);
}
__device__ __forceinline__ f32x16 mma32(u32x4 a, u32x4 b, f32x16 c) {
  return __builtin_amdgcn_mfma_f32_32x32x16_bf16(__builtin_bit_cast(bf16x8, a), __builtin_bit_cast(bf16x8, b), c, 0, 0, 0);
}
__device__ __forceinline__ u32x4 ld128(LDS u8* p) { return *(LDS u32x4*)p; }
__device__ __forceinline__ void bar() {
  asm volatile("s_waitcnt lgkmcnt(0)\n\ts_barrier" ::: "memory");
}

template <int N> struct IC { static constexpr int v = N; };
template <int B, int E, class F> __device__ __forceinline__ void sfor(F&& f) {
  if constexpr (B < E) { f(IC<B>{}); sfor<B + 1, E>(f); }
}

__global__ __launch_bounds__(512, 2) void s4d_fused_kernel(Params P
) {
  __shared__ __attribute__((aligned(256))) u8 smem_[SMEM];
  LDS u8* smem = (LDS u8*)smem_;
  const int tid = threadIdx.x, lane = tid & 63;
  const int w = __builtin_amdgcn_readfirstlane(tid >> 6);
  const int xcd = blockIdx.x & 7, wl = blockIdx.x >> 3;
  const int br = xcd >> 2;
  const int ch0 = (xcd & 3) * 144 + wl * 8;
  const int d = ch0 + w;
  unsigned long long stamps[16];
  STAMP(0);
  RSTAMP(14);
  const uint16_t* pu  = br ? P.p[7]  : P.p[0];
  const uint16_t* pla = br ? P.p[8]  : P.p[1];
  const uint16_t* pai = br ? P.p[9]  : P.p[2];
  const uint16_t* pcr = br ? P.p[10] : P.p[3];
  const uint16_t* pci = br ? P.p[11] : P.p[4];
  const uint16_t* pdt = br ? P.p[12] : P.p[5];
  const uint16_t* pds = br ? P.p[13] : P.p[6];

  const uint32_t g_la = ((const uint32_t*)pla)[d * 64 + lane];
  const uint32_t g_ai = ((const uint32_t*)pai)[d * 64 + lane];
  const uint32_t g_cr = ((const uint32_t*)pcr)[d * 64 + lane];
  const uint32_t g_ci = ((const uint32_t*)pci)[d * 64 + lane];
  const uint32_t g_dt = pdt[d];
  const uint32_t g_ds = pds[d];
  uint32_t pf;
  {
    int idx = min(wl * 342 + min(tid, 341), 6143);
    int row = idx / 3, ln = idx - 3 * row;
    pf = *(const uint32_t*)((const u8*)pu + row * (DCH * 2) + (2 * (xcd & 3) + ln) * 128);
  }

  LDS u8* reg = smem + w * RSTRIDE;
  LDS u8* stg = smem + (w >> 2) * SHALF + (w & 3) * 256;
  LDS u8* ub = smem + UOFF + w * USTRIDE;
  const int c16 = lane & 15, g4 = lane >> 4;
  const int r32 = lane & 31, h2 = lane >> 5;

  if (lane < 60) { u32x2 z = {0, 0}; *(LDS u32x2*)(ub + 8 * lane) = z; }

  int xunit;
  if (r32 < 8) xunit = 65 * r32;
  else if (r32 < 16) xunit = 65 * (r32 - 7);
  else if (r32 < 24) { int t = (int)((0xCBCBAA999999DBBAull >> (8 * (r32 - 16))) & 0xFF); xunit = 64 * (t >> 4) + (t & 15); }
  else if (r32 < 28) xunit = 908 + 129 * (r32 - 24);
  else xunit = 972 + 129 * (r32 - 28);
  LDS u8* xrd = stg + 16 * xunit + 16 * h2;
  LDS u8* yrd = stg + YOFF + 1040 * r32 + 16 * h2;
  int zoff;
  if (lane < 7) zoff = KD0 + 8 * lane;
  else if (lane < 10) zoff = KD0 + 1096 + 8 * (lane - 7);
  else if (lane < 17) zoff = KD1 + 8 * (lane - 10);
  else if (lane < 21) zoff = KD1 + 1088 + 8 * (lane - 17);
  else if (lane < 37) zoff = K4O + 8 * (lane - 21);
  else if (lane < 53) zoff = K4O + 384 + 8 * (lane - 37);
  else zoff = K8O + 256 + 8 * min(lane - 53, 7);

  auto band = [&](int centre, int half) {
    u32x4 r;
    #pragma unroll
    for (int i = 0; i < 4; i++) {
      int k = 8 * g4 + 2 * i - centre - c16;
      uint32_t lo = (k >= -half && k <= half) ? 0x3F80u : 0u;
      uint32_t hi = (k + 1 >= -half && k + 1 <= half) ? 0x3F800000u : 0u;
      r[i] = lo | hi;
    }
    return r;
  };
  const u32x4 b4e = band(8, 2), b4o = band(12, 1), b8e = band(8, 4), b8o = band(8, 3);
  u32x4 cmask;
  #pragma unroll
  for (int i = 0; i < 4; i++) {
    int k = 8 * g4 + 2 * i - c16;
    cmask[i] = ((k == 12 || k == 13) ? 0xFFFFu : 0u) | ((k == 11 || k == 12) ? 0xFFFF0000u : 0u);
  }
  asm volatile("s_mov_b32 m0, %0\n\ts_nop 0" ::"s"((uint32_t)(uintptr_t)stg) : "memory");

  const float dt = rbf(exp_acc(bflo(g_dt)));
  const float dskip = bflo(g_ds);
  STAMP(1);

  u32x4 ur[4];
  {
    const u8* up = (const u8*)pu + (size_t)(256 * w + 4 * lane) * (DCH * 2) + ch0 * 2;
    #pragma unroll
    for (int i = 0; i < 4; i++) ur[i] = *(const u32x4*)(up + i * (DCH * 2));
  }
  asm volatile("" ::"v"(pf));

  f32x16 kacc;
  #pragma unroll
  for (int i = 0; i < 16; i++) kacc[i] = 0.f;
  #pragma unroll
  for (int rd = 0; rd < 2; rd++) {
    const float la = rd ? bfhi(g_la) : bflo(g_la);
    const float ai = rd ? bfhi(g_ai) : bflo(g_ai);
    const float cr = rd ? bfhi(g_cr) : bflo(g_cr);
    const float ci = rd ? bfhi(g_ci) : bflo(g_ci);
    const float a = -rbf(exp_acc(la)) * dt, b = ai * dt;
    const float al = a * 1.44269504088896341f, be = b * 0.159154943091895336f;
    const f32x2 w1 = cpow_direct(al, be, 1.f);
    f32x2 y8 = cpow_direct(al, be, 8.f); y8.y = -y8.y;
    const f32x2 y16 = cmul(y8, y8);
    f32x2 x = {2.f * (cr * dt), 2.f * (ci * dt)};
    f32x2 ye = {1.f, 0.f}, yo = y8;
    f32x2 xs[5];
    xs[0] = x;
    wr_tid<0>(pk2(x));
    sfor<0, 16>([&](auto J) {
      constexpr int j = J.v;
      wr_tid<YOFF + 1040 * (2 * j)>(j == 0 ? 0x00003F80u : pk2(ye));
      wr_tid<YOFF + 1040 * (2 * j + 1)>(pk2(yo));
      ye = cmul(ye, y16);
      if constexpr (j < 15) yo = cmul(yo, y16);
      if constexpr (j < 8) {
        x = cmul(x, w1);
        wr_tid<16 * 65 * (j + 1)>(pk2(x));
        if constexpr (j & 1) xs[(j + 1) / 2] = x;
      }
    });
    const f32x2 w256 = {ye.x, -ye.y};
    f32x2 x256 = cmul(xs[0], w256); wr_tid<16 * 714>(pk2(x256));
    f32x2 x258 = cmul(xs[1], w256); wr_tid<16 * 843>(pk2(x258));
    f32x2 x260 = cmul(xs[2], w256); wr_tid<16 * 585>(pk2(x260));
    f32x2 x262 = cmul(xs[3], w256); wr_tid<16 * 650>(pk2(x262));
    f32x2 x264 = cmul(xs[4], w256); wr_tid<16 * 779>(pk2(x264));
    f32x2 x516 = cmul(x260, w256);  wr_tid<16 * 908>(pk2(x516));
    f32x2 x520 = cmul(x264, w256);  wr_tid<16 * 1037>(pk2(x520));
    f32x2 x772 = cmul(x516, w256);  wr_tid<16 * 1166>(pk2(x772));
    f32x2 x776 = cmul(x520, w256);  wr_tid<16 * 1295>(pk2(x776));
    f32x2 xf = cmul(x776, w256);    wr_tid<16 * 972>(pk2(xf));
    xf = cmul(xf, w256);            wr_tid<16 * 1101>(pk2(xf));
    xf = cmul(xf, w256);            wr_tid<16 * 1230>(pk2(xf));
    xf = cmul(xf, w256);            wr_tid<16 * 1359>(pk2(xf));
    #pragma unroll
    for (int s = 0; s < 8; s++) kacc = mma32(ld128(xrd + 32 * s), ld128(yrd + 32 * s), kacc);
  }
  STAMP(2);

  #pragma unroll
  for (int dd = 0; dd < 4; dd++) {
    u32x2 lo, hi;
    lo.x = __builtin_amdgcn_perm(ur[1][dd], ur[0][dd], 0x05040100u);
    lo.y = __builtin_amdgcn_perm(ur[3][dd], ur[2][dd], 0x05040100u);
    hi.x = __builtin_amdgcn_perm(ur[1][dd], ur[0][dd], 0x07060302u);
    hi.y = __builtin_amdgcn_perm(ur[3][dd], ur[2][dd], 0x07060302u);
    *(LDS u32x2*)(smem + UOFF + (2 * dd) * USTRIDE + 2 * (UPAD + 256 * w + 4 * lane)) = lo;
    *(LDS u32x2*)(smem + UOFF + (2 * dd + 1) * USTRIDE + 2 * (UPAD + 256 * w + 4 * lane)) = hi;
  }

  STAMP(3);
  bar();
  STAMP(4);

  {
    u32x2 z = {0, 0};
    asm volatile("" ::: "memory");
    *(LDS u32x2*)(reg + zoff) = z;
    const int xo = 2 * (540 - 8 * r32 - 4 * h2);
    const float k0 = kacc[0] + ((lane == 0) ? dskip : 0.f);
    u32x2 v;
    v.x = pk(kacc[3], kacc[2]); v.y = pk(kacc[1], k0);            *(LDS u32x2*)(reg + KD1 + xo) = v;
    v.x = pk(kacc[7], kacc[6]); v.y = pk(kacc[5], kacc[4]);       *(LDS u32x2*)(reg + KD0 + xo) = v;
    v.x = pk(kacc[10], kacc[9]); v.y = pk(kacc[8], kacc[8]);      *(LDS u32x2*)(reg + KD1 + xo - 512) = v;
    v.x = pk(kacc[10], kacc[10]); v.y = pk(kacc[9], kacc[8]);     *(LDS u32x2*)(reg + KD0 + xo - 512) = v;
    if (lane == 63) {
      v.x = pk(0.f, kacc[10]); v.y = pk(kacc[10], kacc[10]);      *(LDS u32x2*)(reg + KD1 + 56) = v;
      v.x = 0; v.y = pk(kacc[10], kacc[10]);                      *(LDS u32x2*)(reg + KD0 + 56) = v;
    }
    if (lane == 0) { v.x = pk(k0, 0.f); v.y = 0;                  *(LDS u32x2*)(reg + KD0 + 1088) = v; }
    if (h2 == 0) {
      *(LDS uint32_t*)(reg + K4O + 2 * (190 - 2 * r32)) = pk(kacc[13], kacc[12]);
      *(LDS uint32_t*)(reg + K4O + 2 * (126 - 2 * r32)) = pk(kacc[15], kacc[14]);
    } else {
      uint32_t p0 = pk(kacc[12], kacc[13]), p1 = pk(kacc[14], kacc[15]);
      *(LDS uint16_t*)(reg + K8O + 2 * (127 - r32)) = (uint16_t)p0;
      *(LDS uint16_t*)(reg + K8O + 2 * (95 - r32)) = (uint16_t)(p0 >> 16);
      *(LDS uint16_t*)(reg + K8O + 2 * (63 - r32)) = (uint16_t)p1;
      *(LDS uint16_t*)(reg + K8O + 2 * (31 - r32)) = (uint16_t)(p1 >> 16);
    }
  }
  u32x4 kc;
  {
    uint32_t kv = __builtin_amdgcn_readlane(pk(kacc[15], kacc[15]), 31);
    #pragma unroll
    for (int i = 0; i < 4; i++) kc[i] = kv & cmask[i];
  }

  u32x4 kd[17], k4[6], k8[4];
  {
    LDS u8* kb = reg + ((c16 & 1) ? KD1 : KD0) + 4 * (264 - ((c16 + 1) >> 1) + 4 * g4);
    #pragma unroll
    for (int q = 0; q < 17; q++) {
      LDS uint32_t* p = (LDS uint32_t*)(kb - 64 * q);
      kd[q][0] = p[0]; kd[q][1] = p[1]; kd[q][2] = p[2]; kd[q][3] = p[3];
    }
    LDS u8* k4b = reg + K4O + 2 * (316 - 4 * c16 + 8 * g4);
    #pragma unroll
    for (int s = 3; s <= 8; s++) {
      u32x2 lo = *(LDS u32x2*)(k4b - 64 * s), hi = *(LDS u32x2*)(k4b - 64 * s + 8);
      k4[s - 3][0] = lo.x; k4[s - 3][1] = lo.y; k4[s - 3][2] = hi.x; k4[s - 3][3] = hi.y;
    }
    LDS u8* k8b = reg + K8O + 2 * (254 - 2 * c16 + 8 * g4);
    #pragma unroll
    for (int s = 4; s <= 7; s++) {
      LDS uint32_t* p = (LDS uint32_t*)(k8b - 64 * s);
      k8[s - 4][0] = p[0]; k8[s - 4][1] = p[1]; k8[s - 4][2] = p[2]; k8[s - 4][3] = p[3];
    }
  }
  STAMP(8);

  LDS u8* uf = ub + 2 * (UPAD - 16 + 16 * c16 + 8 * g4);
  LDS u8* u4 = ub + 2 * (UPAD - 24 + 8 * c16 + 8 * g4);
  LDS u8* ysw = reg + YSO + 144 * g4 + 8 * c16;
  f32x4 acc[8];
  #pragma unroll
  for (int i = 0; i < 8; i++) acc[i] = f32x4{0.f, 0.f, 0.f, 0.f};
  u32x4 v8[4];
  const f32x4 zero4 = {0.f, 0.f, 0.f, 0.f};
  LDS u8* ytr = smem + ((lane >> 2) & 3) * RSTRIDE + YSO + 144 * (lane & 3) + 8 * g4;
  uint16_t* outp = P.out + (size_t)(16 * g4 + c16) * (2 * DCH) + br * DCH + ch0;

  sfor<0, 8>([&](auto SS) {
    constexpr int S = SS.v;
    sfor<0, 8>([&](auto RR) {
      constexpr int r = 7 - RR.v;
      u32x4 f = ld128(uf + 64 * (8 * S - r));
      acc[S] = mma(kd[r], f, acc[S]);
      if constexpr (S + 1 < 8) acc[S + 1] = mma(kd[8 + r], f, acc[S + 1]);
      if constexpr (r == 0 && S + 2 < 8) acc[S + 2] = mma(kd[16], f, acc[S + 2]);
    });
    if constexpr (S >= 2) {
      sfor<0, 2>([&](auto PP) {
        constexpr int phi = 2 * S - 4 + PP.v;
        u32x4 f = ld128(u4 + 256 * phi);
        f32x4 e = mma(f, b4e, zero4), o = mma(f, b4o, zero4);
        u32x4 v = {pk(e[0], o[0]), pk(e[1], o[1]), pk(e[2], o[2]), pk(e[3], o[3])};
        sfor<0, 3>([&](auto TT) {
          constexpr int T = S + TT.v;
          if constexpr (T < 8) acc[T] = mma(v, k4[2 * T - phi - 3], acc[T]);
        });
      });
    }
    if constexpr (S >= 4) {
      constexpr int phi = S - 4;
      u32x4 fe = ld128(uf + 512 * phi - 16), fo = ld128(uf + 512 * phi);
      f32x4 e = mma(fe, b8e, zero4), o = mma(fo, b8o, zero4);
      v8[phi] = u32x4{pk(e[0], o[0]), pk(e[1], o[1]), pk(e[2], o[2]), pk(e[3], o[3])};
      sfor<0, phi + 1>([&](auto FF) {
        constexpr int f = FF.v;
        acc[S] = mma(v8[f], k8[S - f - 4], acc[S]);
      });
      acc[S] = mma(kc, fo, acc[S]);
    }
    u32x2 y = {pk(acc[S][0], acc[S][1]), pk(acc[S][2], acc[S][3])};
    *(LDS u32x2*)(ysw + 576 * S) = y;
    bar();
    if (w == S) {
      #pragma unroll
      for (int it = 0; it < 4; it++) {
        s16x4 lo = __builtin_amdgcn_ds_read_tr16_b64_v4i16((LDS s16x4*)(ytr + 576 * S + 32 * it));
        s16x4 hi = __builtin_amdgcn_ds_read_tr16_b64_v4i16((LDS s16x4*)(ytr + 576 * S + 32 * it + 4 * RSTRIDE));
        u32x2 l2 = __builtin_bit_cast(u32x2, lo), h2v = __builtin_bit_cast(u32x2, hi);
        u32x4 o4 = {l2.x, l2.y, h2v.x, h2v.y};
        *(u32x4*)(outp + (size_t)(256 * S + 64 * it) * (2 * DCH)) = o4;
      }
    }
    if constexpr (S == 0) { STAMP(5); }
    if constexpr (S == 3) { STAMP(6); }
  });
  STAMP(7);
  STAMP(9);
  RSTAMP(15);
  asm volatile("s_waitcnt vmcnt(0)" ::: "memory");
  STAMP(9);
  if (lane == 0 && (w == 0 || w == 7) && (blockIdx.x % 29 == 0))
    printf("b%d w%d rt0 %llu rt1 %llu : %d %d %d %d %d %d %d %d %d\n", (int)blockIdx.x, w, stamps[14], stamps[15], (int)(stamps[1]-stamps[0]), (int)(stamps[2]-stamps[0]), (int)(stamps[3]-stamps[0]), (int)(stamps[4]-stamps[0]), (int)(stamps[8]-stamps[0]), (int)(stamps[5]-stamps[0]), (int)(stamps[6]-stamps[0]), (int)(stamps[7]-stamps[0]), (int)(stamps[9]-stamps[0]));
}

extern "C" void kernel_launch(void* const* d_in, const int* in_sizes, int n_in,
                              void* d_out, int out_size, void* d_ws, size_t ws_size,
                              hipStream_t stream) {
  Params P;
  for (int i = 0; i < 14; i++) P.p[i] = (const uint16_t*)d_in[i];
  P.out = (uint16_t*)d_out;
  s4d_fused_kernel<<<144, 512, 0, stream>>>(P);
}
```

```cpp
#include <hip/hip_runtime.h>
#include <hip/hip_bf16.h>
#include <stdint.h>
#include <stdio.h>

typedef float f32x2 __attribute__((ext_vector_type(2)));
typedef float f32x4 __attribute__((ext_vector_type(4)));
typedef float f32x16 __attribute__((ext_vector_type(16)));
typedef __bf16 bf16x8 __attribute__((ext_vector_type(8)));
typedef __bf16 bf16x2 __attribute__((ext_vector_type(2)));
typedef uint32_t u32x4 __attribute__((ext_vector_type(4)));
typedef uint32_t u32x2 __attribute__((ext_vector_type(2)));
typedef short s16x4 __attribute__((ext_vector_type(4)));
typedef unsigned char u8;
#define LDS __attribute__((address_space(3)))

#define STAMP(i)
#define RSTAMP(i)

constexpr int SEQ = 2048, DCH = 576, NMODE = 128;
constexpr int UPAD = 240;
constexpr int USTRIDE = (UPAD + SEQ) * 2;
constexpr int UBYTES = 8 * USTRIDE;
constexpr int XREG = 5888, YREG = 8704;
constexpr int YAREA = 8 * XREG;
constexpr int UOFF = YAREA + 8 * YREG;
constexpr int CNT = UOFF + UBYTES;
constexpr int SMEM = CNT + 64;
constexpr int KD0 = 0, KD1 = 1216, K4O = 2336, K8O = 2848, YSO = 3200;

struct Params { const uint16_t* p[14]; uint16_t* out; };

__device__ __forceinline__ float bflo(uint32_t v) { return __uint_as_float(v << 16); }
__device__ __forceinline__ float bfhi(uint32_t v) { return __uint_as_float(v & 0xFFFF0000u); }
__device__ __forceinline__ uint32_t pk(float lo, float hi) {
  f32x2 v = {lo, hi};
  return __builtin_bit_cast(uint32_t, __builtin_convertvector(v, bf16x2));
}
__device__ __forceinline__ uint32_t pk2(f32x2 v) { return __builtin_bit_cast(uint32_t, __builtin_convertvector(v, bf16x2)); }
__device__ __forceinline__ float rbf(float x) { return bflo(pk(x, x)); }
__device__ __forceinline__ float exp_acc(float x) {
  const float L = 1.44269502162933349609375f, Llo = 1.925963033500011e-8f;
  float t = x * L;
  float r = __builtin_fmaf(x, L, -t);
  r = __builtin_fmaf(x, Llo, r);
  float e = __builtin_amdgcn_exp2f(t);
  return __builtin_fmaf(e * r, 0.693147180559945f, e);
}
__device__ __forceinline__ f32x2 cmul(f32x2 x, f32x2 w) {
  f32x2 t = x * w.xx;
  f32x2 s = {-w.y, w.y};
  return __builtin_elementwise_fma(x.yx, s, t);
}
__device__ __forceinline__ f32x2 cpow_direct(float al, float be, float e) {
  float m = __builtin_amdgcn_exp2f(al * e);
  float ph = __builtin_amdgcn_fractf(be * e);
  f32x2 r = {m * __builtin_amdgcn_cosf(ph), m * __builtin_amdgcn_sinf(ph)};
  return r;
}
template <int OFF> __device__ __forceinline__ void wr_tid(uint32_t v) {
  asm volatile("ds_write_addtid_b32 %0 offset:%1" ::"v"(v), "i"(OFF) : "memory");
}
__device__ __forceinline__ f32x4 mma(u32x4 a, u32x4 b, f32x4 c) {
  return __builtin_amdgcn_mfma_f32_16x16x32_bf16(__builtin_bit_cast(bf16x8, a), __builtin_bit_cast(bf16x8, b), c, 0, 0, 0);
}
__device__ __forceinline__ f32x16 mma32(u32x4 a, u32x4 b, f32x16 c) {
  return __builtin_amdgcn_mfma_f32_32x32x16_bf16(__builtin_bit_cast(bf16x8, a), __builtin_bit_cast(bf16x8, b), c, 0, 0, 0);
}
__device__ __forceinline__ u32x4 ld128(LDS u8* p) { return *(LDS u32x4*)p; }
__device__ __forceinline__ void bar() {
  asm volatile("s_waitcnt lgkmcnt(0)\n\ts_barrier" ::: "memory");
}
__device__ __forceinline__ void set_m0(uint32_t v) {
  asm volatile("s_mov_b32 m0, %0\n\ts_nop 0" ::"s"(v) : "memory");
}
template <int OFF> __device__ __forceinline__ void publish(uint32_t addr, uint32_t one) {
  asm volatile("s_mov_b64 exec, 1\n\tds_add_u32 %0, %1 offset:%2\n\ts_mov_b64 exec, -1" ::"v"(addr), "v"(one), "i"(OFF) : "memory");
}
__device__ __forceinline__ void await(volatile LDS uint32_t* p, uint32_t n) {
  while (__builtin_amdgcn_readfirstlane(*p) < n) __builtin_amdgcn_s_sleep(1);
  asm volatile("" ::: "memory");
}
#define FENCE() __builtin_amdgcn_sched_barrier(0)

template <int N> struct IC { static constexpr int v = N; };
template <int B, int E, class F> __device__ __forceinline__ void sfor(F&& f) {
  if constexpr (B < E) { f(IC<B>{}); sfor<B + 1, E>(f); }
}

__global__ __launch_bounds__(512, 2) void s4d_fused_kernel(Params P
) {
  __shared__ __attribute__((aligned(256))) u8 smem_[SMEM];
  LDS u8* smem = (LDS u8*)smem_;
  const int tid = threadIdx.x, lane = tid & 63;
  const int w = __builtin_amdgcn_readfirstlane(tid >> 6);
  const int xcd = blockIdx.x & 7, wl = blockIdx.x >> 3;
  const int br = xcd >> 2;
  const int ch0 = (xcd & 3) * 144 + wl * 8;
  const int d = ch0 + w;
  STAMP(0);
  RSTAMP(14);
  const uint16_t* pu  = br ? P.p[7]  : P.p[0];
  const uint16_t* pla = br ? P.p[8]  : P.p[1];
  const uint16_t* pai = br ? P.p[9]  : P.p[2];
  const uint16_t* pcr = br ? P.p[10] : P.p[3];
  const uint16_t* pci = br ? P.p[11] : P.p[4];
  const uint16_t* pdt = br ? P.p[12] : P.p[5];
  const uint16_t* pds = br ? P.p[13] : P.p[6];

  const uint32_t g_la = ((const uint32_t*)pla)[d * 64 + lane];
  const uint32_t g_ai = ((const uint32_t*)pai)[d * 64 + lane];
  const uint32_t g_cr = ((const uint32_t*)pcr)[d * 64 + lane];
  const uint32_t g_ci = ((const uint32_t*)pci)[d * 64 + lane];
  const uint32_t g_dt = pdt[d];
  const uint32_t g_ds = pds[d];
  u32x4 ur[8];
  if (w >= 4) {
    const u8* up = (const u8*)pu + (size_t)(512 * (w - 4) + 4 * lane) * (DCH * 2) + ch0 * 2;
    #pragma unroll
    for (int i = 0; i < 8; i++) ur[i] = *(const u32x4*)(up + ((i >> 2) * 256 + (i & 3)) * (DCH * 2));
  }
  if (tid < 16) *(LDS uint32_t*)(smem + CNT + 4 * tid) = 0;
  bar();

  LDS u8* reg = smem + YAREA + w * YREG;
  LDS u8* ub = smem + UOFF + w * USTRIDE;
  const int c16 = lane & 15, g4 = lane >> 4;
  const int r32 = lane & 31, h2 = lane >> 5;
  const uint32_t cnta = (uint32_t)(uintptr_t)(smem + CNT), one = 1;

  if (w >= 4) {
    if (lane < 60) {
      u32x2 z = {0, 0};
      *(LDS u32x2*)(smem + UOFF + (2 * w - 8) * USTRIDE + 8 * lane) = z;
      *(LDS u32x2*)(smem + UOFF + (2 * w - 7) * USTRIDE + 8 * lane) = z;
    }
    LDS u8* ut = smem + UOFF + 2 * (UPAD + 512 * (w - 4) + 4 * lane);
    #pragma unroll
    for (int t = 0; t < 2; t++) {
      #pragma unroll
      for (int dd = 0; dd < 4; dd++) {
        u32x2 lo, hi;
        lo.x = __builtin_amdgcn_perm(ur[4 * t + 1][dd], ur[4 * t][dd], 0x05040100u);
        lo.y = __builtin_amdgcn_perm(ur[4 * t + 3][dd], ur[4 * t + 2][dd], 0x05040100u);
        hi.x = __builtin_amdgcn_perm(ur[4 * t + 1][dd], ur[4 * t][dd], 0x07060302u);
        hi.y = __builtin_amdgcn_perm(ur[4 * t + 3][dd], ur[4 * t + 2][dd], 0x07060302u);
        *(LDS u32x2*)(ut + (2 * dd) * USTRIDE + 512 * t) = lo;
        *(LDS u32x2*)(ut + (2 * dd + 1) * USTRIDE + 512 * t) = hi;
      }
    }
    publish<0>(cnta, one);
  }

  int xunit;
  if (r32 < 8) xunit = 17 * r32;
  else if (r32 < 16) xunit = 17 * (r32 - 7);
  else if (r32 < 24) xunit = (int)((0xCBCBAA999999DBBAull >> (8 * (r32 - 16))) & 0xFF);
  else if (r32 < 28) xunit = 236 + 33 * (r32 - 24);
  else xunit = 252 + 33 * (r32 - 28);
  LDS u8* xrd = smem + w * XREG + 16 * xunit + 16 * h2;
  LDS u8* yrd = reg + 272 * r32 + 16 * h2;
  const uint32_t m0x = (uint32_t)(uintptr_t)smem + w * XREG, m0y = (uint32_t)(uintptr_t)smem + w * YREG;
  int zoff;
  if (lane < 7) zoff = KD0 + 8 * lane;
  else if (lane < 10) zoff = KD0 + 1096 + 8 * (lane - 7);
  else if (lane < 17) zoff = KD1 + 8 * (lane - 10);
  else if (lane < 21) zoff = KD1 + 1088 + 8 * (lane - 17);
  else if (lane < 37) zoff = K4O + 8 * (lane - 21);
  else if (lane < 53) zoff = K4O + 384 + 8 * (lane - 37);
  else zoff = K8O + 256 + 8 * min(lane - 53, 7);

  const float dt = rbf(exp_acc(bflo(g_dt)));
  const float dskip = bflo(g_ds);
  STAMP(1);

  f32x16 kacc;
  #pragma unroll
  for (int i = 0; i < 16; i++) kacc[i] = 0.f;
  u32x4 fa[8], fb[8];
  sfor<0, 2>([&](auto RD) {
    constexpr int rd = RD.v;
    const float la = rd ? bfhi(g_la) : bflo(g_la);
    const float ai = rd ? bfhi(g_ai) : bflo(g_ai);
    const float cr = rd ? bfhi(g_cr) : bflo(g_cr);
    const float ci = rd ? bfhi(g_ci) : bflo(g_ci);
    const float a = -rbf(exp_acc(la)) * dt, b = ai * dt;
    const float al = a * 1.44269504088896341f, be = b * 0.159154943091895336f;
    const f32x2 w1 = cpow_direct(al, be, 1.f);
    f32x2 y8 = cpow_direct(al, be, 8.f); y8.y = -y8.y;
    const f32x2 y16 = cmul(y8, y8);
    f32x2 x = {2.f * (cr * dt), 2.f * (ci * dt)};
    f32x2 ye = {1.f, 0.f}, yo = y8;
    f32x2 xs[5];
    uint32_t xp[9];
    xs[0] = x;
    xp[0] = pk2(x);
    set_m0(m0y);
    sfor<0, 16>([&](auto J) {
      constexpr int j = J.v;
      wr_tid<YAREA + 272 * (2 * j)>(j == 0 ? 0x00003F80u : pk2(ye));
      wr_tid<YAREA + 272 * (2 * j + 1)>(pk2(yo));
      if constexpr (rd == 1 && j >= 8) kacc = mma32(fa[j - 8], fb[j - 8], kacc);
      ye = cmul(ye, y16);
      if constexpr (j < 15) yo = cmul(yo, y16);
      if constexpr (j < 8) {
        x = cmul(x, w1);
        xp[j + 1] = pk2(x);
        if constexpr (j & 1) xs[(j + 1) / 2] = x;
      }
      if constexpr (rd == 1) FENCE();
    });
    set_m0(m0x);
    sfor<0, 9>([&](auto J) { wr_tid<16 * 17 * J.v>(xp[J.v]); });
    const f32x2 w256 = {ye.x, -ye.y};
    f32x2 x256 = cmul(xs[0], w256); wr_tid<16 * 186>(pk2(x256));
    f32x2 x258 = cmul(xs[1], w256); wr_tid<16 * 219>(pk2(x258));
    f32x2 x260 = cmul(xs[2], w256); wr_tid<16 * 153>(pk2(x260));
    f32x2 x262 = cmul(xs[3], w256); wr_tid<16 * 170>(pk2(x262));
    f32x2 x264 = cmul(xs[4], w256); wr_tid<16 * 203>(pk2(x264));
    f32x2 x516 = cmul(x260, w256);  wr_tid<16 * 236>(pk2(x516));
    f32x2 x520 = cmul(x264, w256);  wr_tid<16 * 269>(pk2(x520));
    f32x2 x772 = cmul(x516, w256);  wr_tid<16 * 302>(pk2(x772));
    f32x2 x776 = cmul(x520, w256);  wr_tid<16 * 335>(pk2(x776));
    f32x2 xf = cmul(x776, w256);    wr_tid<16 * 252>(pk2(xf));
    xf = cmul(xf, w256);            wr_tid<16 * 285>(pk2(xf));
    xf = cmul(xf, w256);            wr_tid<16 * 318>(pk2(xf));
    xf = cmul(xf, w256);            wr_tid<16 * 351>(pk2(xf));
    #pragma unroll
    for (int s = 0; s < 8; s++) { fa[s] = ld128(xrd + 32 * s); fb[s] = ld128(yrd + 32 * s); }
    FENCE();
  });
  STAMP(2);
  {
    u32x2 z = {0, 0};
    asm volatile("" ::: "memory");
    *(LDS u32x2*)(reg + zoff) = z;
  }
  #pragma unroll
  for (int s = 0; s < 8; s++) kacc = mma32(fa[s], fb[s], kacc);

  auto band = [&](int centre, int half) {
    u32x4 r;
    #pragma unroll
    for (int i = 0; i < 4; i++) {
      int k = 8 * g4 + 2 * i - centre - c16;
      uint32_t lo = (k >= -half && k <= half) ? 0x3F80u : 0u;
      uint32_t hi = (k + 1 >= -half && k + 1 <= half) ? 0x3F800000u : 0u;
      r[i] = lo | hi;
    }
    return r;
  };
  const u32x4 b4e = band(8, 2), b4o = band(12, 1), b8e = band(8, 4), b8o = band(8, 3);
  u32x4 cmask;
  #pragma unroll
  for (int i = 0; i < 4; i++) {
    int k = 8 * g4 + 2 * i - c16;
    cmask[i] = ((k == 12 || k == 13) ? 0xFFFFu : 0u) | ((k == 11 || k == 12) ? 0xFFFF0000u : 0u);
  }
  STAMP(3);

  {
    const int xo = 2 * (540 - 8 * r32 - 4 * h2);
    const float k0 = kacc[0] + ((lane == 0) ? dskip : 0.f);
    u32x2 v;
    v.x = pk(kacc[3], kacc[2]); v.y = pk(kacc[1], k0);            *(LDS u32x2*)(reg + KD1 + xo) = v;
    v.x = pk(kacc[7], kacc[6]); v.y = pk(kacc[5], kacc[4]);       *(LDS u32x2*)(reg + KD0 + xo) = v;
    v.x = pk(kacc[10], kacc[9]); v.y = pk(kacc[8], kacc[8]);      *(LDS u32x2*)(reg + KD1 + xo - 512) = v;
    v.x = pk(kacc[10], kacc[10]); v.y = pk(kacc[9], kacc[8]);     *(LDS u32x2*)(reg + KD0 + xo - 512) = v;
    if (lane == 63) {
      v.x = pk(0.f, kacc[10]); v.y = pk(kacc[10], kacc[10]);      *(LDS u32x2*)(reg + KD1 + 56) = v;
      v.x = 0; v.y = pk(kacc[10], kacc[10]);                      *(LDS u32x2*)(reg + KD0 + 56) = v;
    }
    if (lane == 0) { v.x = pk(k0, 0.f); v.y = 0;                  *(LDS u32x2*)(reg + KD0 + 1088) = v; }
    if (h2 == 0) {
      *(LDS uint32_t*)(reg + K4O + 2 * (190 - 2 * r32)) = pk(kacc[13], kacc[12]);
      *(LDS uint32_t*)(reg + K4O + 2 * (126 - 2 * r32)) = pk(kacc[15], kacc[14]);
    } else {
      uint32_t p0 = pk(kacc[12], kacc[13]), p1 = pk(kacc[14], kacc[15]);
      *(LDS uint16_t*)(reg + K8O + 2 * (127 - r32)) = (uint16_t)p0;
      *(LDS uint16_t*)(reg + K8O + 2 * (95 - r32)) = (uint16_t)(p0 >> 16);
      *(LDS uint16_t*)(reg + K8O + 2 * (63 - r32)) = (uint16_t)p1;
      *(LDS uint16_t*)(reg + K8O + 2 * (31 - r32)) = (uint16_t)(p1 >> 16);
    }
  }
  u32x4 kc;
  {
    uint32_t kv = __builtin_amdgcn_readlane(pk(kacc[15], kacc[15]), 31);
    #pragma unroll
    for (int i = 0; i < 4; i++) kc[i] = kv & cmask[i];
  }
  STAMP(4);

  u32x4 kd[17], k4[6], k8[4];
  {
    LDS u8* kb = reg + ((c16 & 1) ? KD1 : KD0) + 4 * (264 - ((c16 + 1) >> 1) + 4 * g4);
    #pragma unroll
    for (int q = 0; q < 17; q++) {
      LDS uint32_t* p = (LDS uint32_t*)(kb - 64 * q);
      kd[q][0] = p[0]; kd[q][1] = p[1]; kd[q][2] = p[2]; kd[q][3] = p[3];
    }
    LDS u8* k4b = reg + K4O + 2 * (316 - 4 * c16 + 8 * g4);
    #pragma unroll
    for (int s = 3; s <= 8; s++) {
      u32x2 lo = *(LDS u32x2*)(k4b - 64 * s), hi = *(LDS u32x2*)(k4b - 64 * s + 8);
      k4[s - 3][0] = lo.x; k4[s - 3][1] = lo.y; k4[s - 3][2] = hi.x; k4[s - 3][3] = hi.y;
    }
    LDS u8* k8b = reg + K8O + 2 * (254 - 2 * c16 + 8 * g4);
    #pragma unroll
    for (int s = 4; s <= 7; s++) {
      LDS uint32_t* p = (LDS uint32_t*)(k8b - 64 * s);
      k8[s - 4][0] = p[0]; k8[s - 4][1] = p[1]; k8[s - 4][2] = p[2]; k8[s - 4][3] = p[3];
    }
  }
  await((volatile LDS uint32_t*)(smem + CNT), 4);
  STAMP(8);

  LDS u8* uf = ub + 2 * (UPAD - 16 + 16 * c16 + 8 * g4);
  LDS u8* u4 = ub + 2 * (UPAD - 24 + 8 * c16 + 8 * g4);
  LDS u8* ysw = reg + YSO + 64 * (w & 3) + 144 * g4 + 8 * c16;
  f32x4 acc[8];
  const f32x4 zero4 = {0.f, 0.f, 0.f, 0.f};
  #pragma unroll
  for (int i = 0; i < 8; i++) acc[i] = zero4;
  u32x4 df[64], f4[12], fe[4], fo[4], v8[4], v4a, v4b;
  f32x4 e4a, o4a, e4b, o4b, e8, o8;
  auto cvt8 = [&](f32x4 e, f32x4 o) { return u32x4{pk(e[0], o[0]), pk(e[1], o[1]), pk(e[2], o[2]), pk(e[3], o[3])}; };
  auto finish = [&](auto TT) {
    constexpr int T = TT.v;
    u32x2 y = {pk(acc[T][0], acc[T][1]), pk(acc[T][2], acc[T][3])};
    *(LDS u32x2*)(ysw + 576 * T) = y;
    publish<4 + 4 * T>(cnta, one);
  };
  constexpr int PF = 3;
  #pragma unroll
  for (int n = 0; n < PF; n++) df[n] = ld128(uf + 64 * (n - 7));
  FENCE();
  sfor<0, 64>([&](auto NN) {
    constexpr int n = NN.v, S = n >> 3, k = n & 7, r = 7 - k;
    if constexpr (n + PF < 64) df[n + PF] = ld128(uf + 64 * (n + PF - 7));
    if constexpr (S >= 1 && S < 7 && (k == 4 || k == 5)) f4[2 * S - 6 + k] = ld128(u4 + 256 * (2 * S - 6 + k));
    if constexpr (S >= 3 && S < 7 && k == 6) fe[S - 3] = ld128(uf + 512 * (S - 3) - 16);
    if constexpr (S >= 3 && S < 7 && k == 7) fo[S - 3] = ld128(uf + 512 * (S - 3));
    if constexpr (S >= 2 && k == 0) { e4a = mma(f4[2 * S - 4], b4e, zero4); o4a = mma(f4[2 * S - 4], b4o, zero4); }
    if constexpr (S >= 2 && k == 1) { e4b = mma(f4[2 * S - 3], b4e, zero4); o4b = mma(f4[2 * S - 3], b4o, zero4); }
    if constexpr (S >= 4 && k == 2) {
      e8 = mma(fe[S - 4], b8e, zero4); o8 = mma(fo[S - 4], b8o, zero4);
      acc[S] = mma(kc, fo[S - 4], acc[S]);
    }
    acc[S] = mma(kd[r], df[n], acc[S]);
    if constexpr (S + 1 < 8) acc[S + 1] = mma(kd[8 + r], df[n], acc[S + 1]);
    if constexpr (r == 0 && S + 2 < 8) acc[S + 2] = mma(kd[16], df[n], acc[S + 2]);
    if constexpr (S >= 2 && (k == 3 || k == 4)) {
      constexpr int phi = 2 * S - 7 + k;
      sfor<0, 3>([&](auto TT) {
        constexpr int T = S + TT.v;
        if constexpr (T < 8) acc[T] = mma(k == 3 ? v4a : v4b, k4[2 * T - phi - 3], acc[T]);
      });
    }
    if constexpr (S >= 4 && k == 5)
      sfor<0, S - 4>([&](auto FF) { acc[S] = mma(v8[FF.v], k8[S - FF.v - 4], acc[S]); });
    if constexpr (S >= 4 && k == 6) acc[S] = mma(v8[S - 4], k8[0], acc[S]);
    if constexpr (S >= 1 && k == 1) finish(IC<S - 1>{});
    if constexpr (S >= 2 && k == 2) v4a = cvt8(e4a, o4a);
    if constexpr (S >= 2 && k == 3) v4b = cvt8(e4b, o4b);
    if constexpr (S >= 4 && k == 4) v8[S - 4] = cvt8(e8, o8);
    FENCE();
    if constexpr (n == 7) { STAMP(5); }
    if constexpr (n == 31) { STAMP(6); }
  });
  finish(IC<7>{});
  STAMP(10);

  if (w < 4) {
    LDS u8* ytr = smem + YAREA + ((lane >> 2) & 3) * (YREG + 64) + YSO + 144 * (lane & 3) + 8 * g4 + 32 * w;
    uint16_t* outp = P.out + (size_t)(64 * w + 16 * g4 + c16) * (2 * DCH) + br * DCH + ch0;
    #pragma unroll 1
    for (int S = 0; S < 8; S++) {
      await((volatile LDS uint32_t*)(smem + CNT + 4 + 4 * S), 8);
      s16x4 lo = __builtin_amdgcn_ds_read_tr16_b64_v4i16((LDS s16x4*)(ytr + 576 * S));
      s16x4 hi = __builtin_amdgcn_ds_read_tr16_b64_v4i16((LDS s16x4*)(ytr + 576 * S + 4 * YREG));
      u32x2 l2 = __builtin_bit_cast(u32x2, lo), h2v = __builtin_bit_cast(u32x2, hi);
      u32x4 o4 = {l2.x, l2.y, h2v.x, h2v.y};
      *(u32x4*)(outp + (size_t)(256 * S) * (2 * DCH)) = o4;
    }
  }
  STAMP(7);
  STAMP(9);
  RSTAMP(15);
}

extern "C" void kernel_launch(void* const* d_in, const int* in_sizes, int n_in,
                              void* d_out, int out_size, void* d_ws, size_t ws_size,
                              hipStream_t stream) {
  Params P;
  for (int i = 0; i < 14; i++) P.p[i] = (const uint16_t*)d_in[i];
  P.out = (uint16_t*)d_out;
  s4d_fused_kernel<<<144, 512, 0, stream>>>(P);
}
```

```cpp
#include <hip/hip_runtime.h>
#include <hip/hip_bf16.h>
#include <stdint.h>
#include <stdio.h>

typedef float f32x2 __attribute__((ext_vector_type(2)));
typedef float f32x4 __attribute__((ext_vector_type(4)));
typedef float f32x16 __attribute__((ext_vector_type(16)));
typedef __bf16 bf16x8 __attribute__((ext_vector_type(8)));
typedef __bf16 bf16x2 __attribute__((ext_vector_type(2)));
typedef uint32_t u32x4 __attribute__((ext_vector_type(4)));
typedef uint32_t u32x2 __attribute__((ext_vector_type(2)));
typedef short s16x4 __attribute__((ext_vector_type(4)));
typedef unsigned char u8;
#define LDS __attribute__((address_space(3)))

#define STAMP(i)
#define RSTAMP(i)

constexpr int SEQ = 2048, DCH = 576, NMODE = 128;
constexpr int UPAD = 240;
constexpr int USTRIDE = (UPAD + SEQ) * 2;
constexpr int UBYTES = 8 * USTRIDE;
constexpr int XREG = 5888, YREG = 8704;
constexpr int YAREA = 8 * XREG;
constexpr int UOFF = YAREA + 8 * YREG;
constexpr int CNT = UOFF + UBYTES;
constexpr int SMEM = CNT + 64;
constexpr int KD0 = 0, KD1 = 1408, K4O = 2528, K8O = 3040, YSO = 3392;

struct LaneTab { uint32_t v[6][64][4]; };
constexpr uint32_t band_el(int k, int half) { return (k >= -half && k <= half) ? 0x3F80u : 0u; }
constexpr LaneTab make_tab() {
  LaneTab t{};
  for (int lane = 0; lane < 64; lane++) {
    const int c16 = lane & 15, g4 = lane >> 4, r32 = lane & 31, h2 = lane >> 5;
    const int centre[4] = {8, 12, 8, 8}, half[4] = {2, 1, 4, 3};
    for (int m = 0; m < 4; m++)
      for (int i = 0; i < 4; i++) {
        const int k = 8 * g4 + 2 * i - centre[m] - c16;
        t.v[m][lane][i] = band_el(k, half[m]) | (band_el(k + 1, half[m]) << 16);
      }
    for (int i = 0; i < 4; i++) {
      const int k = 8 * g4 + 2 * i - c16;
      t.v[4][lane][i] = ((k == 12 || k == 13) ? 0xFFFFu : 0u) | ((k == 11 || k == 12) ? 0xFFFF0000u : 0u);
    }
    const int xt[8] = {186, 219, 153, 153, 153, 170, 203, 203};
    const int xunit = r32 < 8 ? 17 * r32 : r32 < 16 ? 17 * (r32 - 7) : r32 < 24 ? xt[r32 - 16]
                    : r32 < 28 ? 236 + 33 * (r32 - 24) : 252 + 33 * (r32 - 28);
    const int zoff = lane < 7 ? KD0 + 8 * lane : lane < 10 ? KD0 + 1096 + 8 * (lane - 7)
                   : lane < 17 ? KD1 + 8 * (lane - 10) : lane < 21 ? KD1 + 1088 + 8 * (lane - 17)
                   : lane < 37 ? K4O + 8 * (lane - 21) : lane < 53 ? K4O + 384 + 8 * (lane - 37)
                   : K8O + 256 + 8 * (lane - 53 < 7 ? lane - 53 : 7);
    t.v[5][lane][0] = 16 * xunit + 16 * h2;
    t.v[5][lane][1] = zoff;
    t.v[5][lane][2] = ((c16 & 1) ? KD1 : KD0) + 4 * (264 - ((c16 + 1) >> 1) + 4 * g4);
    t.v[5][lane][3] = YAREA + ((lane >> 2) & 3) * (YREG + 64) + YSO + 144 * (lane & 3) + 8 * g4;
  }
  return t;
}
__device__ const LaneTab lane_tab = make_tab();

struct Params { const uint16_t* p[14]; uint16_t* out; };

__device__ __forceinline__ float bflo(uint32_t v) { return __uint_as_float(v << 16); }
__device__ __forceinline__ float bfhi(uint32_t v) { return __uint_as_float(v & 0xFFFF0000u); }
__device__ __forceinline__ uint32_t pk(float lo, float hi) {
  f32x2 v = {lo, hi};
  return __builtin_bit_cast(uint32_t, __builtin_convertvector(v, bf16x2));
}
__device__ __forceinline__ uint32_t pk2(f32x2 v) { return __builtin_bit_cast(uint32_t, __builtin_convertvector(v, bf16x2)); }
__device__ __forceinline__ float rbf(float x) { return bflo(pk(x, x)); }
__device__ __forceinline__ float exp_acc(float x) {
  const float L = 1.44269502162933349609375f, Llo = 1.925963033500011e-8f;
  float t = x * L;
  float r = __builtin_fmaf(x, L, -t);
  r = __builtin_fmaf(x, Llo, r);
  float e = __builtin_amdgcn_exp2f(t);
  return __builtin_fmaf(e * r, 0.693147180559945f, e);
}
__device__ __forceinline__ f32x2 cmul(f32x2 x, f32x2 w) {
  f32x2 t = x * w.xx;
  f32x2 s = {-w.y, w.y};
  return __builtin_elementwise_fma(x.yx, s, t);
}
__device__ __forceinline__ f32x2 cm1(f32x2 x, f32x2 w) {
  f32x2 t;
  asm volatile("v_pk_mul_f32 %0, %1, %2 op_sel_hi:[1,0]" : "=v"(t) : "v"(x), "v"(w));
  return t;
}
__device__ __forceinline__ f32x2 cm2(f32x2 x, f32x2 w, f32x2 t) {
  f32x2 r;
  asm volatile("v_pk_fma_f32 %0, %1, %2, %3 op_sel:[1,1,0] op_sel_hi:[0,1,1] neg_lo:[0,1,0]" : "=v"(r) : "v"(x), "v"(w), "v"(t));
  return r;
}
__device__ __forceinline__ f32x2 cm2c(f32x2 x, f32x2 w, f32x2 t) {
  f32x2 r;
  asm volatile("v_pk_fma_f32 %0, %1, %2, %3 op_sel:[1,1,0] op_sel_hi:[0,1,1] neg_hi:[0,1,0]" : "=v"(r) : "v"(x), "v"(w), "v"(t));
  return r;
}
__device__ __forceinline__ f32x2 cpow_direct(float al, float be, float e) {
  float m = __builtin_amdgcn_exp2f(al * e);
  float ph = __builtin_amdgcn_fractf(be * e);
  f32x2 r = {m * __builtin_amdgcn_cosf(ph), m * __builtin_amdgcn_sinf(ph)};
  return r;
}
template <int OFF> __device__ __forceinline__ void wr_tid(uint32_t v) {
  asm volatile("ds_write_addtid_b32 %0 offset:%1" ::"v"(v), "i"(OFF) : "memory");
}
__device__ __forceinline__ f32x4 mma(u32x4 a, u32x4 b, f32x4 c) {
  return __builtin_amdgcn_mfma_f32_16x16x32_bf16(__builtin_bit_cast(bf16x8, a), __builtin_bit_cast(bf16x8, b), c, 0, 0, 0);
}
__device__ __forceinline__ f32x16 mma32(u32x4 a, u32x4 b, f32x16 c) {
  return __builtin_amdgcn_mfma_f32_32x32x16_bf16(__builtin_bit_cast(bf16x8, a), __builtin_bit_cast(bf16x8, b), c, 0, 0, 0);
}
__device__ __forceinline__ u32x4 ld128(LDS u8* p) { return *(LDS u32x4*)p; }
__device__ __forceinline__ void bar() {
  asm volatile("s_waitcnt lgkmcnt(0)\n\ts_barrier" ::: "memory");
}
__device__ __forceinline__ void set_m0(uint32_t v) {
  asm volatile("s_mov_b32 m0, %0\n\ts_nop 0" ::"s"(v) : "memory");
}
template <int OFF> __device__ __forceinline__ void publish(uint32_t addr, uint32_t one) {
  asm volatile("s_mov_b64 exec, 1\n\tds_add_u32 %0, %1 offset:%2\n\ts_mov_b64 exec, -1" ::"v"(addr), "v"(one), "i"(OFF) : "memory");
}
__device__ __forceinline__ void await(volatile LDS uint32_t* p, uint32_t n) {
  while (__builtin_amdgcn_readfirstlane(*p) < n) __builtin_amdgcn_s_sleep(1);
  asm volatile("" ::: "memory");
}
#define FENCE() __builtin_amdgcn_sched_barrier(0)
#define PRIO(x) __builtin_amdgcn_s_setprio(x)

template <int N> struct IC { static constexpr int v = N; };
template <int B, int E, class F> __device__ __forceinline__ void sfor(F&& f) {
  if constexpr (B < E) { f(IC<B>{}); sfor<B + 1, E>(f); }
}

__global__ __launch_bounds__(512, 2) void s4d_fused_kernel(Params P
) {
  __shared__ __attribute__((aligned(256))) u8 smem_[SMEM];
  LDS u8* smem = (LDS u8*)smem_;
  const int tid = threadIdx.x, lane = tid & 63;
  const int w = __builtin_amdgcn_readfirstlane(tid >> 6);
  const int xcd = blockIdx.x & 7, wl = blockIdx.x >> 3;
  const int br = xcd >> 2;
  const int ch0 = (xcd & 3) * 144 + wl * 8;
  const int d = ch0 + w;
  STAMP(0);
  RSTAMP(14);
  const uint16_t* pu  = br ? P.p[7]  : P.p[0];
  const uint16_t* pla = br ? P.p[8]  : P.p[1];
  const uint16_t* pai = br ? P.p[9]  : P.p[2];
  const uint16_t* pcr = br ? P.p[10] : P.p[3];
  const uint16_t* pci = br ? P.p[11] : P.p[4];
  const uint16_t* pdt = br ? P.p[12] : P.p[5];
  const uint16_t* pds = br ? P.p[13] : P.p[6];

  PRIO(3);
  uint16_t* pout = P.out;
  asm volatile("" : "+s"(pout));
  const uint32_t g_la = ((const uint32_t*)pla)[d * 64 + lane];
  const uint32_t g_ai = ((const uint32_t*)pai)[d * 64 + lane];
  const uint32_t g_cr = ((const uint32_t*)pcr)[d * 64 + lane];
  const uint32_t g_ci = ((const uint32_t*)pci)[d * 64 + lane];
  const uint32_t g_dt = pdt[d];
  const uint32_t g_ds = pds[d];
  const u32x4 lt = *(const u32x4*)lane_tab.v[5][lane];
  const u32x4 b4e = *(const u32x4*)lane_tab.v[0][lane], b4o = *(const u32x4*)lane_tab.v[1][lane];
  const u32x4 b8e = *(const u32x4*)lane_tab.v[2][lane], b8o = *(const u32x4*)lane_tab.v[3][lane];
  const u32x4 cmask = *(const u32x4*)lane_tab.v[4][lane];
  if (tid < 16) *(LDS uint32_t*)(smem + CNT + 4 * tid) = 0;
  FENCE();
  bar();
  u32x4 ur[4];
  {
    const u8* up = (const u8*)pu + (size_t)(256 * w + 4 * lane) * (DCH * 2) + ch0 * 2;
    #pragma unroll
    for (int i = 0; i < 4; i++) ur[i] = *(const u32x4*)(up + i * (DCH * 2));
  }
  FENCE();

  LDS u8* reg = smem + YAREA + w * YREG;
  LDS u8* ub = smem + UOFF + w * USTRIDE;
  const int c16 = lane & 15, g4 = lane >> 4;
  const int r32 = lane & 31, h2 = lane >> 5;
  const uint32_t cnta = (uint32_t)(uintptr_t)(smem + CNT), one = 1;

  if (lane < 60) { u32x2 z = {0, 0}; *(LDS u32x2*)(ub + 8 * lane) = z; }

  LDS u8* xrd;
  LDS u8* yrd = reg + 272 * r32 + 16 * h2;
  const uint32_t m0x = (uint32_t)(uintptr_t)smem + w * XREG, m0y = (uint32_t)(uintptr_t)smem + w * YREG;

  const float dt = rbf(exp_acc(bflo(g_dt)));
  const float dskip = bflo(g_ds);
  STAMP(1);

  f32x16 kacc;
  #pragma unroll
  for (int i = 0; i < 16; i++) kacc[i] = 0.f;
  u32x4 fa[8], fb[8];
  sfor<0, 2>([&](auto RD) {
    constexpr int rd = RD.v;
    if constexpr (rd == 1) PRIO(1);
    const float la = rd ? bfhi(g_la) : bflo(g_la);
    const float ai = rd ? bfhi(g_ai) : bflo(g_ai);
    const float cr = rd ? bfhi(g_cr) : bflo(g_cr);
    const float ci = rd ? bfhi(g_ci) : bflo(g_ci);
    const float a = -rbf(exp_acc(la)) * dt, b = ai * dt;
    const float al = a * 1.44269504088896341f, be = b * 0.159154943091895336f;
    const f32x2 w1 = cpow_direct(al, be, 1.f);
    f32x2 y[4], t[5];
    y[1] = cpow_direct(al, be, 8.f); y[1].y = -y[1].y;
    y[0] = f32x2{1.f, 0.f};
    f32x2 x = {2.f * (cr * dt), 2.f * (ci * dt)};
    if constexpr (rd == 1) {
      FENCE();
      #pragma unroll
      for (int s = 0; s < 8; s++) kacc = mma32(fa[s], fb[s], kacc);
      asm volatile("" : "+v"(kacc));
    }
    t[0] = cm1(y[1], y[1]);
    y[2] = cm2(y[1], y[1], t[0]);
    t[0] = cm1(y[2], y[1]); t[1] = cm1(y[2], y[2]);
    y[3] = cm2(y[2], y[1], t[0]);
    const f32x2 y32 = cm2(y[2], y[2], t[1]);
    f32x2 xs[5];
    uint32_t xp[9];
    xs[0] = x;
    xp[0] = pk2(x);
    set_m0(m0y);
    sfor<0, 8>([&](auto I) {
      constexpr int i = I.v;
      if constexpr (i == 4) PRIO(2 - 2 * rd);
      uint32_t p[4];
      sfor<0, 4>([&](auto C) { p[C.v] = (i == 0 && C.v == 0) ? 0x00003F80u : pk2(y[C.v]); });
      t[4] = cm1(x, w1);
      sfor<0, 4>([&](auto C) {
        constexpr int c = C.v;
        if constexpr (i == 0 && c == 0) { }
        else if constexpr (i < 7 || c == 0) t[c] = cm1(y[c], y32);
        wr_tid<YAREA + 272 * (4 * i + c)>(p[c]);
      });
      x = cm2(x, w1, t[4]);
      sfor<0, 4>([&](auto C) {
        constexpr int c = C.v;
        if constexpr (i == 0 && c == 0) y[0] = y32;
        else if constexpr (i < 7 || c == 0) y[c] = cm2(y[c], y32, t[c]);
      });
      xp[i + 1] = pk2(x);
      if constexpr (i & 1) xs[(i + 1) / 2] = x;
      FENCE();
    });
    const f32x2 v256 = y[0];
    set_m0(m0x);
    f32x2 xq[5], v512;
    sfor<0, 5>([&](auto J) { t[J.v] = cm1(xs[J.v], v256); wr_tid<16 * 17 * J.v>(xp[J.v]); });
    f32x2 tv = cm1(v256, v256);
    sfor<0, 4>([&](auto J) { wr_tid<16 * 17 * (5 + J.v)>(xp[5 + J.v]); });
    sfor<0, 5>([&](auto J) { xq[J.v] = cm2c(xs[J.v], v256, t[J.v]); });
    v512 = cm2(v256, v256, tv);
    t[0] = cm1(xq[2], v256); t[1] = cm1(xq[4], v256); t[2] = cm1(xq[2], v512); t[3] = cm1(xq[4], v512);
    wr_tid<16 * 186>(pk2(xq[0])); wr_tid<16 * 219>(pk2(xq[1])); wr_tid<16 * 153>(pk2(xq[2]));
    wr_tid<16 * 170>(pk2(xq[3])); wr_tid<16 * 203>(pk2(xq[4]));
    f32x2 x516 = cm2c(xq[2], v256, t[0]), x520 = cm2c(xq[4], v256, t[1]);
    f32x2 x772 = cm2c(xq[2], v512, t[2]), x776 = cm2c(xq[4], v512, t[3]);
    t[0] = cm1(x520, v512); t[1] = cm1(x776, v512);
    wr_tid<16 * 236>(pk2(x516)); wr_tid<16 * 269>(pk2(x520));
    f32x2 x1032 = cm2c(x520, v512, t[0]), x1288 = cm2c(x776, v512, t[1]);
    wr_tid<16 * 302>(pk2(x772)); wr_tid<16 * 335>(pk2(x776));
    t[0] = cm1(x1032, v512); t[1] = cm1(x1288, v512);
    wr_tid<16 * 252>(pk2(x1032)); wr_tid<16 * 285>(pk2(x1288));
    f32x2 x1544 = cm2c(x1032, v512, t[0]), x1800 = cm2c(x1288, v512, t[1]);
    wr_tid<16 * 318>(pk2(x1544)); wr_tid<16 * 351>(pk2(x1800));
    FENCE();
    if constexpr (rd == 0) xrd = smem + w * XREG + lt[0];
    #pragma unroll
    for (int s = 0; s < 8; s++) { fa[s] = ld128(xrd + 32 * s); fb[s] = ld128(yrd + 32 * s); }
    FENCE();
  });
  STAMP(2);
  {
    u32x2 z = {0, 0};
    asm volatile("" ::: "memory");
    *(LDS u32x2*)(reg + lt[1]) = z;
  }
  {
    LDS u8* ut = smem + UOFF + 2 * (UPAD + 256 * w + 4 * lane);
    #pragma unroll
    for (int dd = 0; dd < 4; dd++) {
      u32x2 lo, hi;
      lo.x = __builtin_amdgcn_perm(ur[1][dd], ur[0][dd], 0x05040100u);
      lo.y = __builtin_amdgcn_perm(ur[3][dd], ur[2][dd], 0x05040100u);
      hi.x = __builtin_amdgcn_perm(ur[1][dd], ur[0][dd], 0x07060302u);
      hi.y = __builtin_amdgcn_perm(ur[3][dd], ur[2][dd], 0x07060302u);
      *(LDS u32x2*)(ut + (2 * dd) * USTRIDE) = lo;
      *(LDS u32x2*)(ut + (2 * dd + 1) * USTRIDE) = hi;
    }
  }
  #pragma unroll
  for (int s = 0; s < 8; s++) kacc = mma32(fa[s], fb[s], kacc);

  {
    const int xo = 2 * (540 - 8 * r32 - 4 * h2);
    const float k0 = kacc[0] + ((lane == 0) ? dskip : 0.f);
    u32x2 v;
    v.x = pk(kacc[3], kacc[2]); v.y = pk(kacc[1], k0);            *(LDS u32x2*)(reg + KD1 + xo) = v;
    v.x = pk(kacc[7], kacc[6]); v.y = pk(kacc[5], kacc[4]);       *(LDS u32x2*)(reg + KD0 + xo) = v;
    v.x = pk(kacc[10], kacc[9]); v.y = pk(kacc[8], kacc[8]);      *(LDS u32x2*)(reg + KD1 + xo - 512) = v;
    v.x = pk(kacc[10], kacc[10]); v.y = pk(kacc[9], kacc[8]);     *(LDS u32x2*)(reg + KD0 + xo - 512) = v;
    if (lane == 63) {
      v.x = pk(0.f, kacc[10]); v.y = pk(kacc[10], kacc[10]);      *(LDS u32x2*)(reg + KD1 + 56) = v;
      v.x = 0; v.y = pk(kacc[10], kacc[10]);                      *(LDS u32x2*)(reg + KD0 + 56) = v;
    }
    if (lane == 0) { v.x = pk(k0, 0.f); v.y = 0;                  *(LDS u32x2*)(reg + KD0 + 1088) = v; }
    if (h2 == 0) {
      *(LDS uint32_t*)(reg + K4O + 2 * (190 - 2 * r32)) = pk(kacc[13], kacc[12]);
      *(LDS uint32_t*)(reg + K4O + 2 * (126 - 2 * r32)) = pk(kacc[15], kacc[14]);
    } else {
      uint32_t p0 = pk(kacc[12], kacc[13]), p1 = pk(kacc[14], kacc[15]);
      *(LDS uint16_t*)(reg + K8O + 2 * (127 - r32)) = (uint16_t)p0;
      *(LDS uint16_t*)(reg + K8O + 2 * (95 - r32)) = (uint16_t)(p0 >> 16);
      *(LDS uint16_t*)(reg + K8O + 2 * (63 - r32)) = (uint16_t)p1;
      *(LDS uint16_t*)(reg + K8O + 2 * (31 - r32)) = (uint16_t)(p1 >> 16);
    }
  }
  u32x4 kc;
  {
    uint32_t kv = __builtin_amdgcn_readlane(pk(kacc[15], kacc[15]), 31);
    #pragma unroll
    for (int i = 0; i < 4; i++) kc[i] = kv & cmask[i];
  }

  u32x4 kd[17], k4[6], k8[4];
  LDS u8* kb = reg + lt[2] - 64 * 16;
  LDS u8* kb2 = kb + 64 * 8;
  LDS u8* k4b = reg + K4O + 2 * (316 - 4 * c16 + 8 * g4) - 64 * 8;
  LDS u8* k4c = k4b + 8;
  LDS u8* k8b = reg + K8O + 2 * (254 - 2 * c16 + 8 * g4) - 64 * 7;
  asm volatile("" : "+v"(kb), "+v"(kb2), "+v"(k4b), "+v"(k4c), "+v"(k8b));
  auto ldkd = [&](auto Q) {
    constexpr int q = Q.v;
    LDS uint32_t* p = (LDS uint32_t*)(q > 8 ? kb + 64 * (16 - q) : kb2 + 64 * (8 - q));
    kd[q][0] = p[0]; kd[q][1] = p[1]; kd[q][2] = p[2]; kd[q][3] = p[3];
  };
  auto ldk4 = [&](auto I) {
    constexpr int s = I.v + 3;
    u32x2 lo = *(LDS u32x2*)(k4b + 64 * (8 - s)), hi = *(LDS u32x2*)(k4c + 64 * (8 - s));
    k4[s - 3][0] = lo.x; k4[s - 3][1] = lo.y; k4[s - 3][2] = hi.x; k4[s - 3][3] = hi.y;
  };
  auto ldk8 = [&](auto I) {
    constexpr int s = I.v + 4;
    LDS uint32_t* p = (LDS uint32_t*)(k8b + 64 * (7 - s));
    k8[s - 4][0] = p[0]; k8[s - 4][1] = p[1]; k8[s - 4][2] = p[2]; k8[s - 4][3] = p[3];
  };
  LDS u8* uf = ub + 2 * (UPAD - 16 + 16 * c16 + 8 * g4);
  LDS u8* u4 = ub + 2 * (UPAD - 24 + 8 * c16 + 8 * g4);
  LDS u8* ysw = reg + YSO + 64 * (w & 3) + 144 * g4 + 8 * c16;
  LDS u8* ytr = smem + lt[3] + 32 * (w & 3);
  uint16_t* outp = pout + (size_t)(64 * (w & 3) + 16 * g4 + c16) * (2 * DCH) + br * DCH + ch0;
  const int grp = w >> 2;
  f32x4 acc[8];
  const f32x4 zero4 = {0.f, 0.f, 0.f, 0.f};
  #pragma unroll
  for (int i = 0; i < 8; i++) acc[i] = zero4;
  u32x4 df[64], f4[12], fe[4], fo[4], v8[4], v4a, v4b;
  f32x4 e4a, o4a, e4b, o4b, e8, o8;
  auto cvt8 = [&](f32x4 e, f32x4 o) { return u32x4{pk(e[0], o[0]), pk(e[1], o[1]), pk(e[2], o[2]), pk(e[3], o[3])}; };
  auto finish = [&](auto TT) {
    constexpr int T = TT.v;
    u32x2 y = {pk(acc[T][0], acc[T][1]), pk(acc[T][2], acc[T][3])};
    *(LDS u32x2*)(ysw + 576 * T) = y;
    publish<8 + 4 * T>(cnta, one);
  };
  uint32_t flag;
  s16x4 tlo, thi;
  auto out_poll = [&](int T) { flag = *(volatile LDS uint32_t*)(smem + CNT + 8 + 4 * T); };
  auto out_read = [&](int T) {
    if (__builtin_amdgcn_readfirstlane(flag) < 8) await((volatile LDS uint32_t*)(smem + CNT + 8 + 4 * T), 8);
    asm volatile("" ::: "memory");
    tlo = __builtin_amdgcn_ds_read_tr16_b64_v4i16((LDS s16x4*)(ytr + 576 * T));
    thi = __builtin_amdgcn_ds_read_tr16_b64_v4i16((LDS s16x4*)(ytr + 576 * T + 4 * YREG));
  };
  auto out_store = [&](int T) {
    u32x2 l2 = __builtin_bit_cast(u32x2, tlo), h2v = __builtin_bit_cast(u32x2, thi);
    u32x4 o4 = {l2.x, l2.y, h2v.x, h2v.y};
    *(u32x4*)(outp + (size_t)(256 * T) * (2 * DCH)) = o4;
  };
  constexpr int PF = 3;
  sfor<0, 8>([&](auto I) { ldkd(IC<7 - I.v>{}); ldkd(IC<15 - I.v>{}); });
  ldkd(IC<16>{});
  FENCE();
  asm volatile("s_waitcnt lgkmcnt(12)\n\ts_barrier" ::: "memory");
  PRIO(3);
  STAMP(8);
  #pragma unroll
  for (int n = 0; n < PF; n++) df[n] = ld128(uf + 64 * (n - 7));
  FENCE();
  sfor<0, 64>([&](auto NN) {
    constexpr int n = NN.v, S = n >> 3, k = n & 7, r = 7 - k;
    if constexpr (n == 24) PRIO(2);
    if constexpr (n == 40) PRIO(1);
    if constexpr (n == 56) PRIO(0);
    if constexpr (n + PF < 64) df[n + PF] = ld128(uf + 64 * (n + PF - 7));
    if constexpr (n >= 4 && n < 10) ldk4(IC<n - 4>{});
    if constexpr (n >= 18 && n < 22) ldk8(IC<n - 18>{});
    if constexpr (S >= 1 && S < 7 && (k == 4 || k == 5)) f4[2 * S - 6 + k] = ld128(u4 + 256 * (2 * S - 6 + k));
    if constexpr (S >= 3 && S < 7 && k == 6) fe[S - 3] = ld128(uf + 512 * (S - 3) - 16);
    if constexpr (S >= 3 && S < 7 && k == 7) fo[S - 3] = ld128(uf + 512 * (S - 3));
    if constexpr (S >= 2 && k == 4) { if (grp == (S & 1)) out_poll(S - 2); }
    if constexpr (S >= 2 && k == 5) { if (grp == (S & 1)) out_read(S - 2); }
    if constexpr (S >= 2 && k == 7) { if (grp == (S & 1)) out_store(S - 2); }
    if constexpr (S >= 2 && k == 0) { e4a = mma(f4[2 * S - 4], b4e, zero4); o4a = mma(f4[2 * S - 4], b4o, zero4); }
    if constexpr (S >= 2 && k == 1) { e4b = mma(f4[2 * S - 3], b4e, zero4); o4b = mma(f4[2 * S - 3], b4o, zero4); }
    if constexpr (S >= 4 && k == 2) {
      e8 = mma(fe[S - 4], b8e, zero4); o8 = mma(fo[S - 4], b8o, zero4);
      acc[S] = mma(kc, fo[S - 4], acc[S]);
    }
    acc[S] = mma(kd[r], df[n], acc[S]);
    if constexpr (S + 1 < 8) acc[S + 1] = mma(kd[8 + r], df[n], acc[S + 1]);
    if constexpr (r == 0 && S + 2 < 8) acc[S + 2] = mma(kd[16], df[n], acc[S + 2]);
    if constexpr (S >= 2 && (k == 3 || k == 4)) {
      constexpr int phi = 2 * S - 7 + k;
      sfor<0, 3>([&](auto TT) {
        constexpr int T = S + TT.v;
        if constexpr (T < 8) acc[T] = mma(k == 3 ? v4a : v4b, k4[2 * T - phi - 3], acc[T]);
      });
    }
    if constexpr (S >= 4 && k == 5)
      sfor<0, S - 4>([&](auto FF) { acc[S] = mma(v8[FF.v], k8[S - FF.v - 4], acc[S]); });
    if constexpr (S >= 4 && k == 6) acc[S] = mma(v8[S - 4], k8[0], acc[S]);
    if constexpr (S >= 1 && k == 1) finish(IC<S - 1>{});
    if constexpr (S >= 2 && k == 2) v4a = cvt8(e4a, o4a);
    if constexpr (S >= 2 && k == 3) v4b = cvt8(e4b, o4b);
    if constexpr (S >= 4 && k == 4) v8[S - 4] = cvt8(e8, o8);
    FENCE();
    if constexpr (n == 7) { STAMP(5); }
    if constexpr (n == 31) { STAMP(6); }
  });
  finish(IC<7>{});
  STAMP(10);
  out_poll(6 + grp);
  out_read(6 + grp);
  out_store(6 + grp);
  STAMP(7);
  STAMP(9);
  RSTAMP(15);
}

extern "C" void kernel_launch(void* const* d_in, const int* in_sizes, int n_in,
                              void* d_out, int out_size, void* d_ws, size_t ws_size,
                              hipStream_t stream) {
  Params P;
  for (int i = 0; i < 14; i++) P.p[i] = (const uint16_t*)d_in[i];
  P.out = (uint16_t*)d_out;
  s4d_fused_kernel<<<144, 512, 0, stream>>>(P);
}
```

```cpp
#include <hip/hip_runtime.h>
#include <hip/hip_bf16.h>
#include <stdint.h>

typedef float f32x2 __attribute__((ext_vector_type(2)));
typedef float f32x4 __attribute__((ext_vector_type(4)));
typedef float f32x16 __attribute__((ext_vector_type(16)));
typedef __bf16 bf16x8 __attribute__((ext_vector_type(8)));
typedef __bf16 bf16x2 __attribute__((ext_vector_type(2)));
typedef uint32_t u32x4 __attribute__((ext_vector_type(4)));
typedef uint32_t u32x2 __attribute__((ext_vector_type(2)));
typedef short s16x4 __attribute__((ext_vector_type(4)));
typedef unsigned char u8;
#define LDS __attribute__((address_space(3)))


constexpr int SEQ = 2048, DCH = 576, NMODE = 128;
constexpr int UPAD = 240;
constexpr int USTRIDE = (UPAD + SEQ) * 2;
constexpr int UBYTES = 8 * USTRIDE;
constexpr int XREG = 5888, YREG = 8704;
constexpr int YAREA = 8 * XREG;
constexpr int UOFF = YAREA + 8 * YREG;
constexpr int CNT = UOFF + UBYTES;
constexpr int SMEM = CNT + 64;
constexpr int KD0 = 0, KD1 = 1408, K4O = 2528, K8O = 3040, YSO = 3392;

struct LaneTab { uint32_t v[6][64][4]; };
constexpr uint32_t band_el(int k, int half) { return (k >= -half && k <= half) ? 0x3F80u : 0u; }
constexpr LaneTab make_tab() {
  LaneTab t{};
  for (int lane = 0; lane < 64; lane++) {
    const int c16 = lane & 15, g4 = lane >> 4, r32 = lane & 31, h2 = lane >> 5;
    const int centre[4] = {8, 12, 8, 8}, half[4] = {2, 1, 4, 3};
    for (int m = 0; m < 4; m++)
      for (int i = 0; i < 4; i++) {
        const int k = 8 * g4 + 2 * i - centre[m] - c16;
        t.v[m][lane][i] = band_el(k, half[m]) | (band_el(k + 1, half[m]) << 16);
      }
    for (int i = 0; i < 4; i++) {
      const int k = 8 * g4 + 2 * i - c16;
      t.v[4][lane][i] = ((k == 12 || k == 13) ? 0xFFFFu : 0u) | ((k == 11 || k == 12) ? 0xFFFF0000u : 0u);
    }
    const int xt[8] = {186, 219, 153, 153, 153, 170, 203, 203};
    const int xunit = r32 < 8 ? 17 * r32 : r32 < 16 ? 17 * (r32 - 7) : r32 < 24 ? xt[r32 - 16]
                    : r32 < 28 ? 236 + 33 * (r32 - 24) : 252 + 33 * (r32 - 28);
    const int zoff = lane < 7 ? KD0 + 8 * lane : lane < 10 ? KD0 + 1096 + 8 * (lane - 7)
                   : lane < 17 ? KD1 + 8 * (lane - 10) : lane < 21 ? KD1 + 1088 + 8 * (lane - 17)
                   : lane < 37 ? K4O + 8 * (lane - 21) : lane < 53 ? K4O + 384 + 8 * (lane - 37)
                   : K8O + 256 + 8 * (lane - 53 < 7 ? lane - 53 : 7);
    t.v[5][lane][0] = 16 * xunit + 16 * h2;
    t.v[5][lane][1] = zoff;
    t.v[5][lane][2] = ((c16 & 1) ? KD1 : KD0) + 4 * (264 - ((c16 + 1) >> 1) + 4 * g4);
    t.v[5][lane][3] = YAREA + ((lane >> 2) & 3) * (YREG + 64) + YSO + 144 * (lane & 3) + 8 * g4;
  }
  return t;
}
__device__ const LaneTab lane_tab = make_tab();

struct Params { const uint16_t* p[14]; uint16_t* out; };

__device__ __forceinline__ float bflo(uint32_t v) { return __uint_as_float(v << 16); }
__device__ __forceinline__ float bfhi(uint32_t v) { return __uint_as_float(v & 0xFFFF0000u); }
__device__ __forceinline__ uint32_t pk(float lo, float hi) {
  f32x2 v = {lo, hi};
  return __builtin_bit_cast(uint32_t, __builtin_convertvector(v, bf16x2));
}
__device__ __forceinline__ uint32_t pk2(f32x2 v) { return __builtin_bit_cast(uint32_t, __builtin_convertvector(v, bf16x2)); }
__device__ __forceinline__ float rbf(float x) { return bflo(pk(x, x)); }
__device__ __forceinline__ float exp_acc(float x) {
  const float L = 1.44269502162933349609375f, Llo = 1.925963033500011e-8f;
  float t = x * L;
  float r = __builtin_fmaf(x, L, -t);
  r = __builtin_fmaf(x, Llo, r);
  float e = __builtin_amdgcn_exp2f(t);
  return __builtin_fmaf(e * r, 0.693147180559945f, e);
}
__device__ __forceinline__ f32x2 cmul(f32x2 x, f32x2 w) {
  f32x2 t = x * w.xx;
  f32x2 s = {-w.y, w.y};
  return __builtin_elementwise_fma(x.yx, s, t);
}
__device__ __forceinline__ f32x2 cm1(f32x2 x, f32x2 w) {
  f32x2 t;
  asm volatile("v_pk_mul_f32 %0, %1, %2 op_sel_hi:[1,0]" : "=v"(t) : "v"(x), "v"(w));
  return t;
}
__device__ __forceinline__ f32x2 cm2(f32x2 x, f32x2 w, f32x2 t) {
  f32x2 r;
  asm volatile("v_pk_fma_f32 %0, %1, %2, %3 op_sel:[1,1,0] op_sel_hi:[0,1,1] neg_lo:[0,1,0]" : "=v"(r) : "v"(x), "v"(w), "v"(t));
  return r;
}
__device__ __forceinline__ f32x2 cm2c(f32x2 x, f32x2 w, f32x2 t) {
  f32x2 r;
  asm volatile("v_pk_fma_f32 %0, %1, %2, %3 op_sel:[1,1,0] op_sel_hi:[0,1,1] neg_hi:[0,1,0]" : "=v"(r) : "v"(x), "v"(w), "v"(t));
  return r;
}
__device__ __forceinline__ f32x2 cpow_direct(float al, float be, float e) {
  float m = __builtin_amdgcn_exp2f(al * e);
  float ph = __builtin_amdgcn_fractf(be * e);
  f32x2 r = {m * __builtin_amdgcn_cosf(ph), m * __builtin_amdgcn_sinf(ph)};
  return r;
}
template <int OFF> __device__ __forceinline__ void wr_tid(uint32_t v) {
  asm volatile("ds_write_addtid_b32 %0 offset:%1" ::"v"(v), "i"(OFF) : "memory");
}
__device__ __forceinline__ f32x4 mma(u32x4 a, u32x4 b, f32x4 c) {
  return __builtin_amdgcn_mfma_f32_16x16x32_bf16(__builtin_bit_cast(bf16x8, a), __builtin_bit_cast(bf16x8, b), c, 0, 0, 0);
}
__device__ __forceinline__ f32x16 mma32(u32x4 a, u32x4 b, f32x16 c) {
  return __builtin_amdgcn_mfma_f32_32x32x16_bf16(__builtin_bit_cast(bf16x8, a), __builtin_bit_cast(bf16x8, b), c, 0, 0, 0);
}
__device__ __forceinline__ u32x4 ld128(LDS u8* p) { return *(LDS u32x4*)p; }
__device__ __forceinline__ void bar() {
  asm volatile("s_waitcnt lgkmcnt(0)\n\ts_barrier" ::: "memory");
}
__device__ __forceinline__ void set_m0(uint32_t v) {
  asm volatile("s_mov_b32 m0, %0\n\ts_nop 0" ::"s"(v) : "memory");
}
template <int OFF> __device__ __forceinline__ void publish(uint32_t addr, uint32_t one) {
  asm volatile("s_mov_b64 exec, 1\n\tds_add_u32 %0, %1 offset:%2\n\ts_mov_b64 exec, -1" ::"v"(addr), "v"(one), "i"(OFF) : "memory");
}
__device__ __forceinline__ void await(volatile LDS uint32_t* p, uint32_t n) {
  while (__builtin_amdgcn_readfirstlane(*p) < n) __builtin_amdgcn_s_sleep(1);
  asm volatile("" ::: "memory");
}
#define FENCE() __builtin_amdgcn_sched_barrier(0)
#define PRIO(x) __builtin_amdgcn_s_setprio(x)

template <int N> struct IC { static constexpr int v = N; };
template <int B, int E, class F> __device__ __forceinline__ void sfor(F&& f) {
  if constexpr (B < E) { f(IC<B>{}); sfor<B + 1, E>(f); }
}

__global__ __launch_bounds__(512, 2) void s4d_fused_kernel(Params P
) {
  __shared__ __attribute__((aligned(256))) u8 smem_[SMEM];
  LDS u8* smem = (LDS u8*)smem_;
  const int tid = threadIdx.x, lane = tid & 63;
  const int w = __builtin_amdgcn_readfirstlane(tid >> 6);
  const int xcd = blockIdx.x & 7, wl = blockIdx.x >> 3;
  const int br = xcd >> 2;
  const int ch0 = (xcd & 3) * 144 + wl * 8;
  const int d = ch0 + w;
  const uint16_t* pu  = br ? P.p[7]  : P.p[0];
  const uint16_t* pla = br ? P.p[8]  : P.p[1];
  const uint16_t* pai = br ? P.p[9]  : P.p[2];
  const uint16_t* pcr = br ? P.p[10] : P.p[3];
  const uint16_t* pci = br ? P.p[11] : P.p[4];
  const uint16_t* pdt = br ? P.p[12] : P.p[5];
  const uint16_t* pds = br ? P.p[13] : P.p[6];

  PRIO(3);
  uint16_t* pout = P.out;
  asm volatile("" : "+s"(pout));
  const uint32_t g_la = ((const uint32_t*)pla)[d * 64 + lane];
  const uint32_t g_ai = ((const uint32_t*)pai)[d * 64 + lane];
  const uint32_t g_cr = ((const uint32_t*)pcr)[d * 64 + lane];
  const uint32_t g_ci = ((const uint32_t*)pci)[d * 64 + lane];
  const uint32_t g_dt = pdt[d];
  const uint32_t g_ds = pds[d];
  const u32x4 lt = *(const u32x4*)lane_tab.v[5][lane];
  const u32x4 b4e = *(const u32x4*)lane_tab.v[0][lane], b4o = *(const u32x4*)lane_tab.v[1][lane];
  const u32x4 b8e = *(const u32x4*)lane_tab.v[2][lane], b8o = *(const u32x4*)lane_tab.v[3][lane];
  const u32x4 cmask = *(const u32x4*)lane_tab.v[4][lane];
  if (tid < 16) *(LDS uint32_t*)(smem + CNT + 4 * tid) = 0;
  FENCE();
  bar();
  u32x4 ur[4];
  {
    const u8* up = (const u8*)pu + (size_t)(256 * w + 4 * lane) * (DCH * 2) + ch0 * 2;
    #pragma unroll
    for (int i = 0; i < 4; i++) ur[i] = *(const u32x4*)(up + i * (DCH * 2));
  }
  FENCE();

  LDS u8* reg = smem + YAREA + w * YREG;
  LDS u8* ub = smem + UOFF + w * USTRIDE;
  const int c16 = lane & 15, g4 = lane >> 4;
  const int r32 = lane & 31, h2 = lane >> 5;
  const uint32_t cnta = (uint32_t)(uintptr_t)(smem + CNT), one = 1;

  if (lane < 60) { u32x2 z = {0, 0}; *(LDS u32x2*)(ub + 8 * lane) = z; }

  LDS u8* xrd;
  LDS u8* yrd = reg + 272 * r32 + 16 * h2;
  const uint32_t m0x = (uint32_t)(uintptr_t)smem + w * XREG, m0y = (uint32_t)(uintptr_t)smem + w * YREG;

  const float dt = rbf(exp_acc(bflo(g_dt)));
  const float dskip = bflo(g_ds);

  f32x16 kacc;
  #pragma unroll
  for (int i = 0; i < 16; i++) kacc[i] = 0.f;
  u32x4 fa[8], fb[8];
  sfor<0, 2>([&](auto RD) {
    constexpr int rd = RD.v;
    if constexpr (rd == 1) PRIO(1);
    const float la = rd ? bfhi(g_la) : bflo(g_la);
    const float ai = rd ? bfhi(g_ai) : bflo(g_ai);
    const float cr = rd ? bfhi(g_cr) : bflo(g_cr);
    const float ci = rd ? bfhi(g_ci) : bflo(g_ci);
    const float a = -rbf(exp_acc(la)) * dt, b = ai * dt;
    const float al = a * 1.44269504088896341f, be = b * 0.159154943091895336f;
    const f32x2 w1 = cpow_direct(al, be, 1.f);
    f32x2 y[4], t[5];
    y[1] = cpow_direct(al, be, 8.f); y[1].y = -y[1].y;
    y[0] = f32x2{1.f, 0.f};
    f32x2 x = {2.f * (cr * dt), 2.f * (ci * dt)};
    if constexpr (rd == 1) {
      FENCE();
      #pragma unroll
      for (int s = 0; s < 8; s++) kacc = mma32(fa[s], fb[s], kacc);
      asm volatile("" : "+v"(kacc));
    }
    t[0] = cm1(y[1], y[1]);
    y[2] = cm2(y[1], y[1], t[0]);
    t[0] = cm1(y[2], y[1]); t[1] = cm1(y[2], y[2]);
    y[3] = cm2(y[2], y[1], t[0]);
    const f32x2 y32 = cm2(y[2], y[2], t[1]);
    f32x2 xs[5];
    uint32_t xp[9];
    xs[0] = x;
    xp[0] = pk2(x);
    set_m0(m0y);
    sfor<0, 8>([&](auto I) {
      constexpr int i = I.v;
      if constexpr (i == 4) PRIO(2 - 2 * rd);
      uint32_t p[4];
      sfor<0, 4>([&](auto C) { p[C.v] = (i == 0 && C.v == 0) ? 0x00003F80u : pk2(y[C.v]); });
      t[4] = cm1(x, w1);
      sfor<0, 4>([&](auto C) {
        constexpr int c = C.v;
        if constexpr (i == 0 && c == 0) { }
        else if constexpr (i < 7 || c == 0) t[c] = cm1(y[c], y32);
        wr_tid<YAREA + 272 * (4 * i + c)>(p[c]);
      });
      x = cm2(x, w1, t[4]);
      sfor<0, 4>([&](auto C) {
        constexpr int c = C.v;
        if constexpr (i == 0 && c == 0) y[0] = y32;
        else if constexpr (i < 7 || c == 0) y[c] = cm2(y[c], y32, t[c]);
      });
      xp[i + 1] = pk2(x);
      if constexpr (i & 1) xs[(i + 1) / 2] = x;
      FENCE();
    });
    const f32x2 v256 = y[0];
    set_m0(m0x);
    f32x2 xq[5], v512;
    sfor<0, 5>([&](auto J) { t[J.v] = cm1(xs[J.v], v256); wr_tid<16 * 17 * J.v>(xp[J.v]); });
    f32x2 tv = cm1(v256, v256);
    sfor<0, 4>([&](auto J) { wr_tid<16 * 17 * (5 + J.v)>(xp[5 + J.v]); });
    sfor<0, 5>([&](auto J) { xq[J.v] = cm2c(xs[J.v], v256, t[J.v]); });
    v512 = cm2(v256, v256, tv);
    t[0] = cm1(xq[2], v256); t[1] = cm1(xq[4], v256); t[2] = cm1(xq[2], v512); t[3] = cm1(xq[4], v512);
    wr_tid<16 * 186>(pk2(xq[0])); wr_tid<16 * 219>(pk2(xq[1])); wr_tid<16 * 153>(pk2(xq[2]));
    wr_tid<16 * 170>(pk2(xq[3])); wr_tid<16 * 203>(pk2(xq[4]));
    f32x2 x516 = cm2c(xq[2], v256, t[0]), x520 = cm2c(xq[4], v256, t[1]);
    f32x2 x772 = cm2c(xq[2], v512, t[2]), x776 = cm2c(xq[4], v512, t[3]);
    t[0] = cm1(x520, v512); t[1] = cm1(x776, v512);
    wr_tid<16 * 236>(pk2(x516)); wr_tid<16 * 269>(pk2(x520));
    f32x2 x1032 = cm2c(x520, v512, t[0]), x1288 = cm2c(x776, v512, t[1]);
    wr_tid<16 * 302>(pk2(x772)); wr_tid<16 * 335>(pk2(x776));
    t[0] = cm1(x1032, v512); t[1] = cm1(x1288, v512);
    wr_tid<16 * 252>(pk2(x1032)); wr_tid<16 * 285>(pk2(x1288));
    f32x2 x1544 = cm2c(x1032, v512, t[0]), x1800 = cm2c(x1288, v512, t[1]);
    wr_tid<16 * 318>(pk2(x1544)); wr_tid<16 * 351>(pk2(x1800));
    FENCE();
    if constexpr (rd == 0) xrd = smem + w * XREG + lt[0];
    #pragma unroll
    for (int s = 0; s < 8; s++) { fa[s] = ld128(xrd + 32 * s); fb[s] = ld128(yrd + 32 * s); }
    FENCE();
  });
  {
    u32x2 z = {0, 0};
    asm volatile("" ::: "memory");
    *(LDS u32x2*)(reg + lt[1]) = z;
  }
  {
    LDS u8* ut = smem + UOFF + 2 * (UPAD + 256 * w + 4 * lane);
    #pragma unroll
    for (int dd = 0; dd < 4; dd++) {
      u32x2 lo, hi;
      lo.x = __builtin_amdgcn_perm(ur[1][dd], ur[0][dd], 0x05040100u);
      lo.y = __builtin_amdgcn_perm(ur[3][dd], ur[2][dd], 0x05040100u);
      hi.x = __builtin_amdgcn_perm(ur[1][dd], ur[0][dd], 0x07060302u);
      hi.y = __builtin_amdgcn_perm(ur[3][dd], ur[2][dd], 0x07060302u);
      *(LDS u32x2*)(ut + (2 * dd) * USTRIDE) = lo;
      *(LDS u32x2*)(ut + (2 * dd + 1) * USTRIDE) = hi;
    }
  }
  #pragma unroll
  for (int s = 0; s < 8; s++) kacc = mma32(fa[s], fb[s], kacc);

  {
    const int xo = 2 * (540 - 8 * r32 - 4 * h2);
    const float k0 = kacc[0] + ((lane == 0) ? dskip : 0.f);
    u32x2 v;
    v.x = pk(kacc[3], kacc[2]); v.y = pk(kacc[1], k0);            *(LDS u32x2*)(reg + KD1 + xo) = v;
    v.x = pk(kacc[7], kacc[6]); v.y = pk(kacc[5], kacc[4]);       *(LDS u32x2*)(reg + KD0 + xo) = v;
    v.x = pk(kacc[10], kacc[9]); v.y = pk(kacc[8], kacc[8]);      *(LDS u32x2*)(reg + KD1 + xo - 512) = v;
    v.x = pk(kacc[10], kacc[10]); v.y = pk(kacc[9], kacc[8]);     *(LDS u32x2*)(reg + KD0 + xo - 512) = v;
    if (lane == 63) {
      v.x = pk(0.f, kacc[10]); v.y = pk(kacc[10], kacc[10]);      *(LDS u32x2*)(reg + KD1 + 56) = v;
      v.x = 0; v.y = pk(kacc[10], kacc[10]);                      *(LDS u32x2*)(reg + KD0 + 56) = v;
    }
    if (lane == 0) { v.x = pk(k0, 0.f); v.y = 0;                  *(LDS u32x2*)(reg + KD0 + 1088) = v; }
    if (h2 == 0) {
      *(LDS uint32_t*)(reg + K4O + 2 * (190 - 2 * r32)) = pk(kacc[13], kacc[12]);
      *(LDS uint32_t*)(reg + K4O + 2 * (126 - 2 * r32)) = pk(kacc[15], kacc[14]);
    } else {
      uint32_t p0 = pk(kacc[12], kacc[13]), p1 = pk(kacc[14], kacc[15]);
      *(LDS uint16_t*)(reg + K8O + 2 * (127 - r32)) = (uint16_t)p0;
      *(LDS uint16_t*)(reg + K8O + 2 * (95 - r32)) = (uint16_t)(p0 >> 16);
      *(LDS uint16_t*)(reg + K8O + 2 * (63 - r32)) = (uint16_t)p1;
      *(LDS uint16_t*)(reg + K8O + 2 * (31 - r32)) = (uint16_t)(p1 >> 16);
    }
  }
  u32x4 kc;
  {
    uint32_t kv = __builtin_amdgcn_readlane(pk(kacc[15], kacc[15]), 31);
    #pragma unroll
    for (int i = 0; i < 4; i++) kc[i] = kv & cmask[i];
  }

  u32x4 kd[17], k4[6], k8[4];
  LDS u8* kb = reg + lt[2] - 64 * 16;
  LDS u8* kb2 = kb + 64 * 8;
  LDS u8* k4b = reg + K4O + 2 * (316 - 4 * c16 + 8 * g4) - 64 * 8;
  LDS u8* k4c = k4b + 8;
  LDS u8* k8b = reg + K8O + 2 * (254 - 2 * c16 + 8 * g4) - 64 * 7;
  asm volatile("" : "+v"(kb), "+v"(kb2), "+v"(k4b), "+v"(k4c), "+v"(k8b));
  auto ldkd = [&](auto Q) {
    constexpr int q = Q.v;
    LDS uint32_t* p = (LDS uint32_t*)(q > 8 ? kb + 64 * (16 - q) : kb2 + 64 * (8 - q));
    kd[q][0] = p[0]; kd[q][1] = p[1]; kd[q][2] = p[2]; kd[q][3] = p[3];
  };
  auto ldk4 = [&](auto I) {
    constexpr int s = I.v + 3;
    u32x2 lo = *(LDS u32x2*)(k4b + 64 * (8 - s)), hi = *(LDS u32x2*)(k4c + 64 * (8 - s));
    k4[s - 3][0] = lo.x; k4[s - 3][1] = lo.y; k4[s - 3][2] = hi.x; k4[s - 3][3] = hi.y;
  };
  auto ldk8 = [&](auto I) {
    constexpr int s = I.v + 4;
    LDS uint32_t* p = (LDS uint32_t*)(k8b + 64 * (7 - s));
    k8[s - 4][0] = p[0]; k8[s - 4][1] = p[1]; k8[s - 4][2] = p[2]; k8[s - 4][3] = p[3];
  };
  LDS u8* uf = ub + 2 * (UPAD - 16 + 16 * c16 + 8 * g4);
  LDS u8* u4 = ub + 2 * (UPAD - 24 + 8 * c16 + 8 * g4);
  LDS u8* ysw = reg + YSO + 64 * (w & 3) + 144 * g4 + 8 * c16;
  LDS u8* ytr = smem + lt[3] + 32 * (w & 3);
  uint16_t* outp = pout + (size_t)(64 * (w & 3) + 16 * g4 + c16) * (2 * DCH) + br * DCH + ch0;
  const int grp = w >> 2;
  f32x4 acc[8];
  const f32x4 zero4 = {0.f, 0.f, 0.f, 0.f};
  #pragma unroll
  for (int i = 0; i < 8; i++) acc[i] = zero4;
  u32x4 df[64], f4[12], fe[4], fo[4], v8[4], v4a, v4b;
  f32x4 e4a, o4a, e4b, o4b, e8, o8;
  auto cvt8 = [&](f32x4 e, f32x4 o) { return u32x4{pk(e[0], o[0]), pk(e[1], o[1]), pk(e[2], o[2]), pk(e[3], o[3])}; };
  auto finish = [&](auto TT) {
    constexpr int T = TT.v;
    u32x2 y = {pk(acc[T][0], acc[T][1]), pk(acc[T][2], acc[T][3])};
    *(LDS u32x2*)(ysw + 576 * T) = y;
    publish<8 + 4 * T>(cnta, one);
  };
  uint32_t flag;
  s16x4 tlo, thi;
  auto out_poll = [&](int T) { flag = *(volatile LDS uint32_t*)(smem + CNT + 8 + 4 * T); };
  auto out_read = [&](int T) {
    if (__builtin_amdgcn_readfirstlane(flag) < 8) await((volatile LDS uint32_t*)(smem + CNT + 8 + 4 * T), 8);
    asm volatile("" ::: "memory");
    tlo = __builtin_amdgcn_ds_read_tr16_b64_v4i16((LDS s16x4*)(ytr + 576 * T));
    thi = __builtin_amdgcn_ds_read_tr16_b64_v4i16((LDS s16x4*)(ytr + 576 * T + 4 * YREG));
  };
  auto out_store = [&](int T) {
    u32x2 l2 = __builtin_bit_cast(u32x2, tlo), h2v = __builtin_bit_cast(u32x2, thi);
    u32x4 o4 = {l2.x, l2.y, h2v.x, h2v.y};
    *(u32x4*)(outp + (size_t)(256 * T) * (2 * DCH)) = o4;
  };
  constexpr int PF = 3;
  sfor<0, 8>([&](auto I) { ldkd(IC<7 - I.v>{}); ldkd(IC<15 - I.v>{}); });
  ldkd(IC<16>{});
  FENCE();
  asm volatile("s_waitcnt lgkmcnt(12)\n\ts_barrier" ::: "memory");
  PRIO(3);
  #pragma unroll
  for (int n = 0; n < PF; n++) df[n] = ld128(uf + 64 * (n - 7));
  FENCE();
  sfor<0, 64>([&](auto NN) {
    constexpr int n = NN.v, S = n >> 3, k = n & 7, r = 7 - k;
    if constexpr (n == 24) PRIO(2);
    if constexpr (n == 40) PRIO(1);
    if constexpr (n == 56) PRIO(0);
    if constexpr (n + PF < 64) df[n + PF] = ld128(uf + 64 * (n + PF - 7));
    if constexpr (n >= 4 && n < 10) ldk4(IC<n - 4>{});
    if constexpr (n >= 18 && n < 22) ldk8(IC<n - 18>{});
    if constexpr (S >= 1 && S < 7 && (k == 4 || k == 5)) f4[2 * S - 6 + k] = ld128(u4 + 256 * (2 * S - 6 + k));
    if constexpr (S >= 3 && S < 7 && k == 6) fe[S - 3] = ld128(uf + 512 * (S - 3) - 16);
    if constexpr (S >= 3 && S < 7 && k == 7) fo[S - 3] = ld128(uf + 512 * (S - 3));
    if constexpr (S >= 2 && k == 4) { if (grp == (S & 1)) out_poll(S - 2); }
    if constexpr (S >= 2 && k == 5) { if (grp == (S & 1)) out_read(S - 2); }
    if constexpr (S >= 2 && k == 7) { if (grp == (S & 1)) out_store(S - 2); }
    if constexpr (S >= 2 && k == 0) { e4a = mma(f4[2 * S - 4], b4e, zero4); o4a = mma(f4[2 * S - 4], b4o, zero4); }
    if constexpr (S >= 2 && k == 1) { e4b = mma(f4[2 * S - 3], b4e, zero4); o4b = mma(f4[2 * S - 3], b4o, zero4); }
    if constexpr (S >= 4 && k == 2) {
      e8 = mma(fe[S - 4], b8e, zero4); o8 = mma(fo[S - 4], b8o, zero4);
      acc[S] = mma(kc, fo[S - 4], acc[S]);
    }
    acc[S] = mma(kd[r], df[n], acc[S]);
    if constexpr (S + 1 < 8) acc[S + 1] = mma(kd[8 + r], df[n], acc[S + 1]);
    if constexpr (r == 0 && S + 2 < 8) acc[S + 2] = mma(kd[16], df[n], acc[S + 2]);
    if constexpr (S >= 2 && (k == 3 || k == 4)) {
      constexpr int phi = 2 * S - 7 + k;
      sfor<0, 3>([&](auto TT) {
        constexpr int T = S + TT.v;
        if constexpr (T < 8) acc[T] = mma(k == 3 ? v4a : v4b, k4[2 * T - phi - 3], acc[T]);
      });
    }
    if constexpr (S >= 4 && k == 5)
      sfor<0, S - 4>([&](auto FF) { acc[S] = mma(v8[FF.v], k8[S - FF.v - 4], acc[S]); });
    if constexpr (S >= 4 && k == 6) acc[S] = mma(v8[S - 4], k8[0], acc[S]);
    if constexpr (S >= 1 && k == 1) finish(IC<S - 1>{});
    if constexpr (S >= 2 && k == 2) v4a = cvt8(e4a, o4a);
    if constexpr (S >= 2 && k == 3) v4b = cvt8(e4b, o4b);
    if constexpr (S >= 4 && k == 4) v8[S - 4] = cvt8(e8, o8);
    FENCE();
  });
  finish(IC<7>{});
  out_poll(6 + grp);
  out_read(6 + grp);
  out_store(6 + grp);
}

extern "C" void kernel_launch(void* const* d_in, const int* in_sizes, int n_in,
                              void* d_out, int out_size, void* d_ws, size_t ws_size,
                              hipStream_t stream) {
  Params P;
  for (int i = 0; i < 14; i++) P.p[i] = (const uint16_t*)d_in[i];
  P.out = (uint16_t*)d_out;
  s4d_fused_kernel<<<144, 512, 0, stream>>>(P);
}
```

```cpp
#include <hip/hip_runtime.h>
#include <hip/hip_bf16.h>
#include <stdint.h>
#include <stdio.h>

typedef float f32x2 __attribute__((ext_vector_type(2)));
typedef float f32x4 __attribute__((ext_vector_type(4)));
typedef float f32x16 __attribute__((ext_vector_type(16)));
typedef __bf16 bf16x8 __attribute__((ext_vector_type(8)));
typedef __bf16 bf16x2 __attribute__((ext_vector_type(2)));
typedef uint32_t u32x4 __attribute__((ext_vector_type(4)));
typedef uint32_t u32x2 __attribute__((ext_vector_type(2)));
typedef short s16x4 __attribute__((ext_vector_type(4)));
typedef unsigned char u8;
#define LDS __attribute__((address_space(3)))
#define GLB __attribute__((address_space(1)))

#define STAMP(i)
#define RSTAMP(i)

#define USLOT 0
constexpr int SEQ = 2048, DCH = 576, NMODE = 128;
constexpr int UPAD = 240;
constexpr int USTRIDE = (UPAD + SEQ) * 2;
constexpr int UBYTES = 8 * USTRIDE;
constexpr int XREG = 5888, YREG = 8704;
constexpr int YAREA = 8 * XREG;
constexpr int UOFF = YAREA + 8 * YREG;
constexpr int CNT = UOFF + UBYTES;
constexpr int SMEM = CNT + 64;
constexpr int KD0 = 0, KD1 = 1408, K4O = 2528, K8O = 3040, YSO = 3392;

struct LaneTab { uint32_t v[6][64][4]; };
constexpr uint32_t band_el(int k, int half) { return (k >= -half && k <= half) ? 0x3F80u : 0u; }
constexpr LaneTab make_tab() {
  LaneTab t{};
  for (int lane = 0; lane < 64; lane++) {
    const int c16 = lane & 15, g4 = lane >> 4, r32 = lane & 31, h2 = lane >> 5;
    const int centre[4] = {8, 12, 8, 8}, half[4] = {2, 1, 4, 3};
    for (int m = 0; m < 4; m++)
      for (int i = 0; i < 4; i++) {
        const int k = 8 * g4 + 2 * i - centre[m] - c16;
        t.v[m][lane][i] = band_el(k, half[m]) | (band_el(k + 1, half[m]) << 16);
      }
    for (int i = 0; i < 4; i++) {
      const int k = 8 * g4 + 2 * i - c16;
      t.v[4][lane][i] = ((k == 12 || k == 13) ? 0xFFFFu : 0u) | ((k == 11 || k == 12) ? 0xFFFF0000u : 0u);
    }
    const int xt[8] = {186, 219, 153, 153, 153, 170, 203, 203};
    const int xunit = r32 < 8 ? 17 * r32 : r32 < 16 ? 17 * (r32 - 7) : r32 < 24 ? xt[r32 - 16]
                    : r32 < 28 ? 236 + 33 * (r32 - 24) : 252 + 33 * (r32 - 28);
    const int zoff = lane < 7 ? KD0 + 8 * lane : lane < 10 ? KD0 + 1096 + 8 * (lane - 7)
                   : lane < 17 ? KD1 + 8 * (lane - 10) : lane < 21 ? KD1 + 1088 + 8 * (lane - 17)
                   : lane < 37 ? K4O + 8 * (lane - 21) : lane < 53 ? K4O + 384 + 8 * (lane - 37)
                   : K8O + 256 + 8 * (lane - 53 < 7 ? lane - 53 : 7);
    t.v[5][lane][0] = 16 * xunit + 16 * h2;
    t.v[5][lane][1] = zoff;
    t.v[5][lane][2] = ((c16 & 1) ? KD1 : KD0) + 4 * (264 - ((c16 + 1) >> 1) + 4 * g4);
    t.v[5][lane][3] = YAREA + ((lane >> 2) & 3) * (YREG + 64) + YSO + 144 * (lane & 3) + 8 * g4;
  }
  return t;
}
__device__ const LaneTab lane_tab = make_tab();

struct Params { const uint16_t* p[14]; uint16_t* out; };

__device__ __forceinline__ float bflo(uint32_t v) { return __uint_as_float(v << 16); }
__device__ __forceinline__ float bfhi(uint32_t v) { return __uint_as_float(v & 0xFFFF0000u); }
__device__ __forceinline__ uint32_t pk(float lo, float hi) {
  f32x2 v = {lo, hi};
  return __builtin_bit_cast(uint32_t, __builtin_convertvector(v, bf16x2));
}
__device__ __forceinline__ uint32_t pk2(f32x2 v) { return __builtin_bit_cast(uint32_t, __builtin_convertvector(v, bf16x2)); }
__device__ __forceinline__ float rbf(float x) { return bflo(pk(x, x)); }
__device__ __forceinline__ float exp_acc(float x) {
  const float L = 1.44269502162933349609375f, Llo = 1.925963033500011e-8f;
  float t = x * L;
  float r = __builtin_fmaf(x, L, -t);
  r = __builtin_fmaf(x, Llo, r);
  float e = __builtin_amdgcn_exp2f(t);
  return __builtin_fmaf(e * r, 0.693147180559945f, e);
}
__device__ __forceinline__ f32x2 cmul(f32x2 x, f32x2 w) {
  f32x2 t = x * w.xx;
  f32x2 s = {-w.y, w.y};
  return __builtin_elementwise_fma(x.yx, s, t);
}
__device__ __forceinline__ f32x2 cm1(f32x2 x, f32x2 w) {
  f32x2 t;
  asm volatile("v_pk_mul_f32 %0, %1, %2 op_sel_hi:[1,0]" : "=v"(t) : "v"(x), "v"(w));
  return t;
}
__device__ __forceinline__ f32x2 cm2(f32x2 x, f32x2 w, f32x2 t) {
  f32x2 r;
  asm volatile("v_pk_fma_f32 %0, %1, %2, %3 op_sel:[1,1,0] op_sel_hi:[0,1,1] neg_lo:[0,1,0]" : "=v"(r) : "v"(x), "v"(w), "v"(t));
  return r;
}
__device__ __forceinline__ f32x2 cm2c(f32x2 x, f32x2 w, f32x2 t) {
  f32x2 r;
  asm volatile("v_pk_fma_f32 %0, %1, %2, %3 op_sel:[1,1,0] op_sel_hi:[0,1,1] neg_hi:[0,1,0]" : "=v"(r) : "v"(x), "v"(w), "v"(t));
  return r;
}
__device__ __forceinline__ f32x2 cpow_direct(float al, float be, float e) {
  float m = __builtin_amdgcn_exp2f(al * e);
  float ph = __builtin_amdgcn_fractf(be * e);
  f32x2 r = {m * __builtin_amdgcn_cosf(ph), m * __builtin_amdgcn_sinf(ph)};
  return r;
}
template <int OFF> __device__ __forceinline__ void wr_tid(uint32_t v) {
  asm volatile("ds_write_addtid_b32 %0 offset:%1" ::"v"(v), "i"(OFF) : "memory");
}
__device__ __forceinline__ f32x4 mma(u32x4 a, u32x4 b, f32x4 c) {
  return __builtin_amdgcn_mfma_f32_16x16x32_bf16(__builtin_bit_cast(bf16x8, a), __builtin_bit_cast(bf16x8, b), c, 0, 0, 0);
}
__device__ __forceinline__ f32x16 mma32(u32x4 a, u32x4 b, f32x16 c) {
  return __builtin_amdgcn_mfma_f32_32x32x16_bf16(__builtin_bit_cast(bf16x8, a), __builtin_bit_cast(bf16x8, b), c, 0, 0, 0);
}
__device__ __forceinline__ u32x4 ld128(LDS u8* p) { return *(LDS u32x4*)p; }
__device__ __forceinline__ void bar() {
  asm volatile("s_waitcnt lgkmcnt(0)\n\ts_barrier" ::: "memory");
}
__device__ __forceinline__ void set_m0(uint32_t v) {
  asm volatile("s_mov_b32 m0, %0\n\ts_nop 0" ::"s"(v) : "memory");
}
template <int OFF> __device__ __forceinline__ void publish(uint32_t addr, uint32_t one) {
  asm volatile("s_mov_b64 exec, 1\n\tds_add_u32 %0, %1 offset:%2\n\ts_mov_b64 exec, -1" ::"v"(addr), "v"(one), "i"(OFF) : "memory");
}
__device__ __forceinline__ void await(volatile LDS uint32_t* p, uint32_t n) {
  while (__builtin_amdgcn_readfirstlane(*p) < n) __builtin_amdgcn_s_sleep(1);
  asm volatile("" ::: "memory");
}
#define FENCE() __builtin_amdgcn_sched_barrier(0)
#define PRIO(x) __builtin_amdgcn_s_setprio(x)

template <int N> struct IC { static constexpr int v = N; };
template <int B, int E, class F> __device__ __forceinline__ void sfor(F&& f) {
  if constexpr (B < E) { f(IC<B>{}); sfor<B + 1, E>(f); }
}

__global__ __launch_bounds__(512, 2) void s4d_fused_kernel(Params P
) {
  __shared__ __attribute__((aligned(256))) u8 smem_[SMEM];
  LDS u8* smem = (LDS u8*)smem_;
  const int tid = threadIdx.x, lane = tid & 63;
  const int w = __builtin_amdgcn_readfirstlane(tid >> 6);
  const int xcd = blockIdx.x & 7, wl = blockIdx.x >> 3;
  const int br = xcd >> 2;
  const int ch0 = (xcd & 3) * 144 + wl * 8;
  const int d = ch0 + w;
  STAMP(0);
  RSTAMP(14);
  uint32_t warm;
  {
    uint64_t pc; uint32_t len;
    asm volatile("s_getpc_b64 %0\n.Ls4d_here:\n\ts_mov_b32 %1, .Ls4d_end-.Ls4d_here" : "=s"(pc), "=s"(len));
    const uint32_t off = min(64u * tid, len - 4);
    warm = *(const GLB uint32_t*)(pc + off);
  }
  FENCE();
  const uint16_t* pu  = br ? P.p[7]  : P.p[0];
  const uint16_t* pla = br ? P.p[8]  : P.p[1];
  const uint16_t* pai = br ? P.p[9]  : P.p[2];
  const uint16_t* pcr = br ? P.p[10] : P.p[3];
  const uint16_t* pci = br ? P.p[11] : P.p[4];
  const uint16_t* pdt = br ? P.p[12] : P.p[5];
  const uint16_t* pds = br ? P.p[13] : P.p[6];

  PRIO(3);
  GLB uint16_t* pout = (GLB uint16_t*)P.out;
  asm volatile("" : "+s"(pout));
  const uint32_t g_la = ((const uint32_t*)pla)[d * 64 + lane];
  const uint32_t g_ai = ((const uint32_t*)pai)[d * 64 + lane];
  const uint32_t g_cr = ((const uint32_t*)pcr)[d * 64 + lane];
  const uint32_t g_ci = ((const uint32_t*)pci)[d * 64 + lane];
  const uint32_t g_dt = pdt[d];
  const uint32_t g_ds = pds[d];
  const u32x4 lt = *(const u32x4*)lane_tab.v[5][lane];
  const u32x4 b4e = *(const u32x4*)lane_tab.v[0][lane], b4o = *(const u32x4*)lane_tab.v[1][lane];
  const u32x4 b8e = *(const u32x4*)lane_tab.v[2][lane], b8o = *(const u32x4*)lane_tab.v[3][lane];
  const u32x4 cmask = *(const u32x4*)lane_tab.v[4][lane];
  if (tid < 16) *(LDS uint32_t*)(smem + CNT + 4 * tid) = 0;
  u32x4 ur[4];
  const u8* up = (const u8*)pu + (size_t)(256 * w + 4 * lane) * (DCH * 2) + ch0 * 2;
  FENCE();

  LDS u8* reg = smem + YAREA + w * YREG;
  LDS u8* ub = smem + UOFF + w * USTRIDE;
  const int c16 = lane & 15, g4 = lane >> 4;
  const int r32 = lane & 31, h2 = lane >> 5;
  const uint32_t cnta = (uint32_t)(uintptr_t)(smem + CNT), one = 1;

  if (lane < 60) { u32x2 z = {0, 0}; *(LDS u32x2*)(ub + 8 * lane) = z; }

  LDS u8* xrd;
  LDS u8* yrd = reg + 272 * r32 + 16 * h2;
  const uint32_t m0x = (uint32_t)(uintptr_t)smem + w * XREG, m0y = (uint32_t)(uintptr_t)smem + w * YREG;

  const float dt = rbf(exp_acc(bflo(g_dt)));
  const float dskip = bflo(g_ds);
  STAMP(1);

  f32x16 kacc;
  #pragma unroll
  for (int i = 0; i < 16; i++) kacc[i] = 0.f;
  u32x4 fa[8], fb[8];
  sfor<0, 2>([&](auto RD) {
    constexpr int rd = RD.v;
    if constexpr (rd == 1) PRIO(1);
    const float la = rd ? bfhi(g_la) : bflo(g_la);
    const float ai = rd ? bfhi(g_ai) : bflo(g_ai);
    const float cr = rd ? bfhi(g_cr) : bflo(g_cr);
    const float ci = rd ? bfhi(g_ci) : bflo(g_ci);
    const float a = -rbf(exp_acc(la)) * dt, b = ai * dt;
    const float al = a * 1.44269504088896341f, be = b * 0.159154943091895336f;
    const f32x2 w1 = cpow_direct(al, be, 1.f);
    f32x2 y[4], t[5];
    y[1] = cpow_direct(al, be, 8.f); y[1].y = -y[1].y;
    y[0] = f32x2{1.f, 0.f};
    f32x2 x = {2.f * (cr * dt), 2.f * (ci * dt)};
    if constexpr (rd == 1) {
      FENCE();
      #pragma unroll
      for (int s = 0; s < 8; s++) kacc = mma32(fa[s], fb[s], kacc);
      asm volatile("" : "+v"(kacc));
    }
    t[0] = cm1(y[1], y[1]);
    y[2] = cm2(y[1], y[1], t[0]);
    t[0] = cm1(y[2], y[1]); t[1] = cm1(y[2], y[2]);
    y[3] = cm2(y[2], y[1], t[0]);
    const f32x2 y32 = cm2(y[2], y[2], t[1]);
    f32x2 xs[5];
    uint32_t xp[9];
    xs[0] = x;
    xp[0] = pk2(x);
    set_m0(m0y);
    sfor<0, 8>([&](auto I) {
      constexpr int i = I.v;
      if constexpr (i == 4) PRIO(2 - 2 * rd);
      uint32_t p[4];
      sfor<0, 4>([&](auto C) { p[C.v] = (i == 0 && C.v == 0) ? 0x00003F80u : pk2(y[C.v]); });
      t[4] = cm1(x, w1);
      sfor<0, 4>([&](auto C) {
        constexpr int c = C.v;
        if constexpr (i == 0 && c == 0) { }
        else if constexpr (i < 7 || c == 0) t[c] = cm1(y[c], y32);
        wr_tid<YAREA + 272 * (4 * i + c)>(p[c]);
      });
      x = cm2(x, w1, t[4]);
      sfor<0, 4>([&](auto C) {
        constexpr int c = C.v;
        if constexpr (i == 0 && c == 0) y[0] = y32;
        else if constexpr (i < 7 || c == 0) y[c] = cm2(y[c], y32, t[c]);
      });
      xp[i + 1] = pk2(x);
      if constexpr (rd == 0 && (i & 1) == USLOT) ur[i >> 1] = *(const u32x4*)(up + (i >> 1) * (DCH * 2));
      if constexpr (i & 1) xs[(i + 1) / 2] = x;
      FENCE();
    });
    const f32x2 v256 = y[0];
    set_m0(m0x);
    f32x2 xq[5], v512;
    sfor<0, 5>([&](auto J) { t[J.v] = cm1(xs[J.v], v256); wr_tid<16 * 17 * J.v>(xp[J.v]); });
    f32x2 tv = cm1(v256, v256);
    sfor<0, 4>([&](auto J) { wr_tid<16 * 17 * (5 + J.v)>(xp[5 + J.v]); });
    sfor<0, 5>([&](auto J) { xq[J.v] = cm2c(xs[J.v], v256, t[J.v]); });
    v512 = cm2(v256, v256, tv);
    t[0] = cm1(xq[2], v256); t[1] = cm1(xq[4], v256); t[2] = cm1(xq[2], v512); t[3] = cm1(xq[4], v512);
    wr_tid<16 * 186>(pk2(xq[0])); wr_tid<16 * 219>(pk2(xq[1])); wr_tid<16 * 153>(pk2(xq[2]));
    wr_tid<16 * 170>(pk2(xq[3])); wr_tid<16 * 203>(pk2(xq[4]));
    f32x2 x516 = cm2c(xq[2], v256, t[0]), x520 = cm2c(xq[4], v256, t[1]);
    f32x2 x772 = cm2c(xq[2], v512, t[2]), x776 = cm2c(xq[4], v512, t[3]);
    t[0] = cm1(x520, v512); t[1] = cm1(x776, v512);
    wr_tid<16 * 236>(pk2(x516)); wr_tid<16 * 269>(pk2(x520));
    f32x2 x1032 = cm2c(x520, v512, t[0]), x1288 = cm2c(x776, v512, t[1]);
    wr_tid<16 * 302>(pk2(x772)); wr_tid<16 * 335>(pk2(x776));
    t[0] = cm1(x1032, v512); t[1] = cm1(x1288, v512);
    wr_tid<16 * 252>(pk2(x1032)); wr_tid<16 * 285>(pk2(x1288));
    f32x2 x1544 = cm2c(x1032, v512, t[0]), x1800 = cm2c(x1288, v512, t[1]);
    wr_tid<16 * 318>(pk2(x1544)); wr_tid<16 * 351>(pk2(x1800));
    FENCE();
    if constexpr (rd == 0) xrd = smem + w * XREG + lt[0];
    #pragma unroll
    for (int s = 0; s < 8; s++) { fa[s] = ld128(xrd + 32 * s); fb[s] = ld128(yrd + 32 * s); }
    FENCE();
  });
  STAMP(2);
  {
    u32x2 z = {0, 0};
    asm volatile("" ::: "memory");
    *(LDS u32x2*)(reg + lt[1]) = z;
  }
  asm volatile("" ::"v"(warm));
  {
    LDS u8* ut = smem + UOFF + 2 * (UPAD + 256 * w + 4 * lane);
    #pragma unroll
    for (int dd = 0; dd < 4; dd++) {
      u32x2 lo, hi;
      lo.x = __builtin_amdgcn_perm(ur[1][dd], ur[0][dd], 0x05040100u);
      lo.y = __builtin_amdgcn_perm(ur[3][dd], ur[2][dd], 0x05040100u);
      hi.x = __builtin_amdgcn_perm(ur[1][dd], ur[0][dd], 0x07060302u);
      hi.y = __builtin_amdgcn_perm(ur[3][dd], ur[2][dd], 0x07060302u);
      *(LDS u32x2*)(ut + (2 * dd) * USTRIDE) = lo;
      *(LDS u32x2*)(ut + (2 * dd + 1) * USTRIDE) = hi;
    }
  }
  #pragma unroll
  for (int s = 0; s < 8; s++) kacc = mma32(fa[s], fb[s], kacc);

  {
    const int xo = 2 * (540 - 8 * r32 - 4 * h2);
    const float k0 = kacc[0] + ((lane == 0) ? dskip : 0.f);
    u32x2 v;
    v.x = pk(kacc[3], kacc[2]); v.y = pk(kacc[1], k0);            *(LDS u32x2*)(reg + KD1 + xo) = v;
    v.x = pk(kacc[7], kacc[6]); v.y = pk(kacc[5], kacc[4]);       *(LDS u32x2*)(reg + KD0 + xo) = v;
    v.x = pk(kacc[10], kacc[9]); v.y = pk(kacc[8], kacc[8]);      *(LDS u32x2*)(reg + KD1 + xo - 512) = v;
    v.x = pk(kacc[10], kacc[10]); v.y = pk(kacc[9], kacc[8]);     *(LDS u32x2*)(reg + KD0 + xo - 512) = v;
    if (lane == 63) {
      v.x = pk(0.f, kacc[10]); v.y = pk(kacc[10], kacc[10]);      *(LDS u32x2*)(reg + KD1 + 56) = v;
      v.x = 0; v.y = pk(kacc[10], kacc[10]);                      *(LDS u32x2*)(reg + KD0 + 56) = v;
    }
    if (lane == 0) { v.x = pk(k0, 0.f); v.y = 0;                  *(LDS u32x2*)(reg + KD0 + 1088) = v; }
    if (h2 == 0) {
      *(LDS uint32_t*)(reg + K4O + 2 * (190 - 2 * r32)) = pk(kacc[13], kacc[12]);
      *(LDS uint32_t*)(reg + K4O + 2 * (126 - 2 * r32)) = pk(kacc[15], kacc[14]);
    } else {
      uint32_t p0 = pk(kacc[12], kacc[13]), p1 = pk(kacc[14], kacc[15]);
      *(LDS uint16_t*)(reg + K8O + 2 * (127 - r32)) = (uint16_t)p0;
      *(LDS uint16_t*)(reg + K8O + 2 * (95 - r32)) = (uint16_t)(p0 >> 16);
      *(LDS uint16_t*)(reg + K8O + 2 * (63 - r32)) = (uint16_t)p1;
      *(LDS uint16_t*)(reg + K8O + 2 * (31 - r32)) = (uint16_t)(p1 >> 16);
    }
  }
  u32x4 kc;
  {
    uint32_t kv = __builtin_amdgcn_readlane(pk(kacc[15], kacc[15]), 31);
    #pragma unroll
    for (int i = 0; i < 4; i++) kc[i] = kv & cmask[i];
  }

  u32x4 kd[17], k4[6], k8[4];
  LDS u8* kb = reg + lt[2] - 64 * 16;
  LDS u8* kb2 = kb + 64 * 8;
  LDS u8* k4b = reg + K4O + 2 * (316 - 4 * c16 + 8 * g4) - 64 * 8;
  LDS u8* k4c = k4b + 8;
  LDS u8* k8b = reg + K8O + 2 * (254 - 2 * c16 + 8 * g4) - 64 * 7;
  asm volatile("" : "+v"(kb), "+v"(kb2), "+v"(k4b), "+v"(k4c), "+v"(k8b));
  auto ldkd = [&](auto Q) {
    constexpr int q = Q.v;
    LDS uint32_t* p = (LDS uint32_t*)(q > 8 ? kb + 64 * (16 - q) : kb2 + 64 * (8 - q));
    kd[q][0] = p[0]; kd[q][1] = p[1]; kd[q][2] = p[2]; kd[q][3] = p[3];
  };
  auto ldk4 = [&](auto I) {
    constexpr int s = I.v + 3;
    u32x2 lo = *(LDS u32x2*)(k4b + 64 * (8 - s)), hi = *(LDS u32x2*)(k4c + 64 * (8 - s));
    k4[s - 3][0] = lo.x; k4[s - 3][1] = lo.y; k4[s - 3][2] = hi.x; k4[s - 3][3] = hi.y;
  };
  auto ldk8 = [&](auto I) {
    constexpr int s = I.v + 4;
    LDS uint32_t* p = (LDS uint32_t*)(k8b + 64 * (7 - s));
    k8[s - 4][0] = p[0]; k8[s - 4][1] = p[1]; k8[s - 4][2] = p[2]; k8[s - 4][3] = p[3];
  };
  LDS u8* uf = ub + 2 * (UPAD - 16 + 16 * c16 + 8 * g4);
  LDS u8* u4 = ub + 2 * (UPAD - 24 + 8 * c16 + 8 * g4);
  LDS u8* ysw = reg + YSO + 64 * (w & 3) + 144 * g4 + 8 * c16;
  LDS u8* ytr = smem + lt[3] + 32 * (w & 3);
  GLB uint16_t* outp = pout + (size_t)(64 * (w & 3) + 16 * g4 + c16) * (2 * DCH) + br * DCH + ch0;
  const int grp = w >> 2;
  f32x4 acc[8];
  const f32x4 zero4 = {0.f, 0.f, 0.f, 0.f};
  #pragma unroll
  for (int i = 0; i < 8; i++) acc[i] = zero4;
  u32x4 df[64], f4[12], fe[4], fo[4], v8[4], v4a, v4b;
  f32x4 e4a, o4a, e4b, o4b, e8, o8;
  auto cvt8 = [&](f32x4 e, f32x4 o) { return u32x4{pk(e[0], o[0]), pk(e[1], o[1]), pk(e[2], o[2]), pk(e[3], o[3])}; };
  auto finish = [&](auto TT) {
    constexpr int T = TT.v;
    u32x2 y = {pk(acc[T][0], acc[T][1]), pk(acc[T][2], acc[T][3])};
    *(LDS u32x2*)(ysw + 576 * T) = y;
    publish<8 + 4 * T>(cnta, one);
  };
  uint32_t flag;
  s16x4 tlo, thi;
  auto out_poll = [&](int T) { flag = *(volatile LDS uint32_t*)(smem + CNT + 8 + 4 * T); };
  auto out_read = [&](int T) {
    if (__builtin_amdgcn_readfirstlane(flag) < 8) await((volatile LDS uint32_t*)(smem + CNT + 8 + 4 * T), 8);
    asm volatile("" ::: "memory");
    tlo = __builtin_amdgcn_ds_read_tr16_b64_v4i16((LDS s16x4*)(ytr + 576 * T));
    thi = __builtin_amdgcn_ds_read_tr16_b64_v4i16((LDS s16x4*)(ytr + 576 * T + 4 * YREG));
  };
  auto out_store = [&](int T) {
    u32x2 l2 = __builtin_bit_cast(u32x2, tlo), h2v = __builtin_bit_cast(u32x2, thi);
    u32x4 o4 = {l2.x, l2.y, h2v.x, h2v.y};
    *(GLB u32x4*)(outp + (size_t)(256 * T) * (2 * DCH)) = o4;
  };
  constexpr int PF = 3;
  sfor<0, 8>([&](auto I) { ldkd(IC<7 - I.v>{}); ldkd(IC<15 - I.v>{}); });
  ldkd(IC<16>{});
  FENCE();
  asm volatile("s_waitcnt lgkmcnt(12)\n\ts_barrier" ::: "memory");
  PRIO(3);
  STAMP(8);
  #pragma unroll
  for (int n = 0; n < PF; n++) df[n] = ld128(uf + 64 * (n - 7));
  FENCE();
  sfor<0, 64>([&](auto NN) {
    constexpr int n = NN.v, S = n >> 3, k = n & 7, r = 7 - k;
    if constexpr (n == 24) PRIO(2);
    if constexpr (n == 40) PRIO(1);
    if constexpr (n == 56) PRIO(0);
    if constexpr (n + PF < 64) df[n + PF] = ld128(uf + 64 * (n + PF - 7));
    if constexpr (n >= 4 && n < 10) ldk4(IC<n - 4>{});
    if constexpr (n >= 18 && n < 22) ldk8(IC<n - 18>{});
    if constexpr (S >= 1 && S < 7 && (k == 4 || k == 5)) f4[2 * S - 6 + k] = ld128(u4 + 256 * (2 * S - 6 + k));
    if constexpr (S >= 3 && S < 7 && k == 6) fe[S - 3] = ld128(uf + 512 * (S - 3) - 16);
    if constexpr (S >= 3 && S < 7 && k == 7) fo[S - 3] = ld128(uf + 512 * (S - 3));
    if constexpr (S >= 2 && k == 4) { if (grp == (S & 1)) out_poll(S - 2); }
    if constexpr (S >= 2 && k == 5) { if (grp == (S & 1)) out_read(S - 2); }
    if constexpr (S >= 2 && k == 7) { if (grp == (S & 1)) out_store(S - 2); }
    if constexpr (S >= 2 && k == 0) { e4a = mma(f4[2 * S - 4], b4e, zero4); o4a = mma(f4[2 * S - 4], b4o, zero4); }
    if constexpr (S >= 2 && k == 1) { e4b = mma(f4[2 * S - 3], b4e, zero4); o4b = mma(f4[2 * S - 3], b4o, zero4); }
    if constexpr (S >= 4 && k == 2) {
      e8 = mma(fe[S - 4], b8e, zero4); o8 = mma(fo[S - 4], b8o, zero4);
      acc[S] = mma(kc, fo[S - 4], acc[S]);
    }
    acc[S] = mma(kd[r], df[n], acc[S]);
    if constexpr (S + 1 < 8) acc[S + 1] = mma(kd[8 + r], df[n], acc[S + 1]);
    if constexpr (r == 0 && S + 2 < 8) acc[S + 2] = mma(kd[16], df[n], acc[S + 2]);
    if constexpr (S >= 2 && (k == 3 || k == 4)) {
      constexpr int phi = 2 * S - 7 + k;
      sfor<0, 3>([&](auto TT) {
        constexpr int T = S + TT.v;
        if constexpr (T < 8) acc[T] = mma(k == 3 ? v4a : v4b, k4[2 * T - phi - 3], acc[T]);
      });
    }
    if constexpr (S >= 4 && k == 5)
      sfor<0, S - 4>([&](auto FF) { acc[S] = mma(v8[FF.v], k8[S - FF.v - 4], acc[S]); });
    if constexpr (S >= 4 && k == 6) acc[S] = mma(v8[S - 4], k8[0], acc[S]);
    if constexpr (S >= 1 && k == 1) finish(IC<S - 1>{});
    if constexpr (S >= 2 && k == 2) v4a = cvt8(e4a, o4a);
    if constexpr (S >= 2 && k == 3) v4b = cvt8(e4b, o4b);
    if constexpr (S >= 4 && k == 4) v8[S - 4] = cvt8(e8, o8);
    FENCE();
    if constexpr (n == 7) { STAMP(5); }
    if constexpr (n == 31) { STAMP(6); }
  });
  finish(IC<7>{});
  STAMP(10);
  out_poll(6 + grp);
  out_read(6 + grp);
  out_store(6 + grp);
  asm volatile(".Ls4d_end:");
  STAMP(7);
  STAMP(9);
  RSTAMP(15);
}

extern "C" void kernel_launch(void* const* d_in, const int* in_sizes, int n_in,
                              void* d_out, int out_size, void* d_ws, size_t ws_size,
                              hipStream_t stream) {
  Params P;
  for (int i = 0; i < 14; i++) P.p[i] = (const uint16_t*)d_in[i];
  P.out = (uint16_t*)d_out;
  s4d_fused_kernel<<<144, 512, 0, stream>>>(P);
}
```

```cpp
#include <hip/hip_runtime.h>
#include <hip/hip_bf16.h>
#include <stdint.h>
#include <stdio.h>

typedef float f32x2 __attribute__((ext_vector_type(2)));
typedef float f32x4 __attribute__((ext_vector_type(4)));
typedef float f32x16 __attribute__((ext_vector_type(16)));
typedef __bf16 bf16x8 __attribute__((ext_vector_type(8)));
typedef __bf16 bf16x2 __attribute__((ext_vector_type(2)));
typedef uint32_t u32x4 __attribute__((ext_vector_type(4)));
typedef uint32_t u32x2 __attribute__((ext_vector_type(2)));
typedef short s16x4 __attribute__((ext_vector_type(4)));
typedef unsigned char u8;
#define LDS __attribute__((address_space(3)))
#define GLB __attribute__((address_space(1)))

#define STAMP(i)
#define RSTAMP(i)

#define TSLOT 2
#define PTRANS 2
#define PFD 3
constexpr int SEQ = 2048, DCH = 576, NMODE = 128;
constexpr int UPAD = 240;
constexpr int USTRIDE = (UPAD + SEQ) * 2;
constexpr int UBYTES = 8 * USTRIDE;
constexpr int XREG = 5888, YREG = 8704;
constexpr int YAREA = 8 * XREG;
constexpr int UOFF = YAREA + 8 * YREG;
constexpr int CNT = UOFF + UBYTES;
constexpr int SMEM = CNT + 64;
constexpr int KD0 = 0, KD1 = 1408, K4O = 2528, K8O = 3040, YSO = 3392;

struct LaneTab { uint32_t v[6][64][4]; };
constexpr uint32_t band_el(int k, int half) { return (k >= -half && k <= half) ? 0x3F80u : 0u; }
constexpr LaneTab make_tab() {
  LaneTab t{};
  for (int lane = 0; lane < 64; lane++) {
    const int c16 = lane & 15, g4 = lane >> 4, r32 = lane & 31, h2 = lane >> 5;
    const int centre[4] = {8, 12, 8, 8}, half[4] = {2, 1, 4, 3};
    for (int m = 0; m < 4; m++)
      for (int i = 0; i < 4; i++) {
        const int k = 8 * g4 + 2 * i - centre[m] - c16;
        t.v[m][lane][i] = band_el(k, half[m]) | (band_el(k + 1, half[m]) << 16);
      }
    for (int i = 0; i < 4; i++) {
      const int k = 8 * g4 + 2 * i - c16;
      t.v[4][lane][i] = ((k == 12 || k == 13) ? 0xFFFFu : 0u) | ((k == 11 || k == 12) ? 0xFFFF0000u : 0u);
    }
    const int xt[8] = {186, 219, 153, 153, 153, 170, 203, 203};
    const int xunit = r32 < 8 ? 17 * r32 : r32 < 16 ? 17 * (r32 - 7) : r32 < 24 ? xt[r32 - 16]
                    : r32 < 28 ? 236 + 33 * (r32 - 24) : 252 + 33 * (r32 - 28);
    const int zoff = lane < 7 ? KD0 + 8 * lane : lane < 10 ? KD0 + 1096 + 8 * (lane - 7)
                   : lane < 17 ? KD1 + 8 * (lane - 10) : lane < 21 ? KD1 + 1088 + 8 * (lane - 17)
                   : lane < 37 ? K4O + 8 * (lane - 21) : lane < 53 ? K4O + 384 + 8 * (lane - 37)
                   : K8O + 256 + 8 * (lane - 53 < 7 ? lane - 53 : 7);
    t.v[5][lane][0] = 16 * xunit + 16 * h2;
    t.v[5][lane][1] = zoff;
    t.v[5][lane][2] = ((c16 & 1) ? KD1 : KD0) + 4 * (264 - ((c16 + 1) >> 1) + 4 * g4);
    t.v[5][lane][3] = YAREA + ((lane >> 2) & 3) * (YREG + 64) + YSO + 144 * (lane & 3) + 8 * g4;
  }
  return t;
}
__device__ const LaneTab lane_tab = make_tab();

struct Params { const uint16_t* p[14]; uint16_t* out; };

__device__ __forceinline__ float bflo(uint32_t v) { return __uint_as_float(v << 16); }
__device__ __forceinline__ float bfhi(uint32_t v) { return __uint_as_float(v & 0xFFFF0000u); }
__device__ __forceinline__ uint32_t pk(float lo, float hi) {
  f32x2 v = {lo, hi};
  return __builtin_bit_cast(uint32_t, __builtin_convertvector(v, bf16x2));
}
__device__ __forceinline__ uint32_t pk2(f32x2 v) { return __builtin_bit_cast(uint32_t, __builtin_convertvector(v, bf16x2)); }
__device__ __forceinline__ float rbf(float x) { return bflo(pk(x, x)); }
__device__ __forceinline__ float exp_acc(float x) {
  const float L = 1.44269502162933349609375f, Llo = 1.925963033500011e-8f;
  float t = x * L;
  float r = __builtin_fmaf(x, L, -t);
  r = __builtin_fmaf(x, Llo, r);
  float e = __builtin_amdgcn_exp2f(t);
  return __builtin_fmaf(e * r, 0.693147180559945f, e);
}
__device__ __forceinline__ f32x2 cmul(f32x2 x, f32x2 w) {
  f32x2 t = x * w.xx;
  f32x2 s = {-w.y, w.y};
  return __builtin_elementwise_fma(x.yx, s, t);
}
__device__ __forceinline__ f32x2 cm1(f32x2 x, f32x2 w) {
  f32x2 t;
  asm volatile("v_pk_mul_f32 %0, %1, %2 op_sel_hi:[1,0]" : "=v"(t) : "v"(x), "v"(w));
  return t;
}
__device__ __forceinline__ f32x2 cm2(f32x2 x, f32x2 w, f32x2 t) {
  f32x2 r;
  asm volatile("v_pk_fma_f32 %0, %1, %2, %3 op_sel:[1,1,0] op_sel_hi:[0,1,1] neg_lo:[0,1,0]" : "=v"(r) : "v"(x), "v"(w), "v"(t));
  return r;
}
__device__ __forceinline__ f32x2 cm2c(f32x2 x, f32x2 w, f32x2 t) {
  f32x2 r;
  asm volatile("v_pk_fma_f32 %0, %1, %2, %3 op_sel:[1,1,0] op_sel_hi:[0,1,1] neg_hi:[0,1,0]" : "=v"(r) : "v"(x), "v"(w), "v"(t));
  return r;
}
__device__ __forceinline__ f32x2 cpow_direct(float al, float be, float e) {
  float m = __builtin_amdgcn_exp2f(al * e);
  float ph = __builtin_amdgcn_fractf(be * e);
  f32x2 r = {m * __builtin_amdgcn_cosf(ph), m * __builtin_amdgcn_sinf(ph)};
  return r;
}
template <int OFF> __device__ __forceinline__ void wr_tid(uint32_t v) {
  asm volatile("ds_write_addtid_b32 %0 offset:%1" ::"v"(v), "i"(OFF) : "memory");
}
__device__ __forceinline__ f32x4 mma(u32x4 a, u32x4 b, f32x4 c) {
  return __builtin_amdgcn_mfma_f32_16x16x32_bf16(__builtin_bit_cast(bf16x8, a), __builtin_bit_cast(bf16x8, b), c, 0, 0, 0);
}
__device__ __forceinline__ f32x16 mma32(u32x4 a, u32x4 b, f32x16 c) {
  return __builtin_amdgcn_mfma_f32_32x32x16_bf16(__builtin_bit_cast(bf16x8, a), __builtin_bit_cast(bf16x8, b), c, 0, 0, 0);
}
__device__ __forceinline__ u32x4 ld128(LDS u8* p) { return *(LDS u32x4*)p; }
__device__ __forceinline__ void bar() {
  asm volatile("s_waitcnt lgkmcnt(0)\n\ts_barrier" ::: "memory");
}
__device__ __forceinline__ void set_m0(uint32_t v) {
  asm volatile("s_mov_b32 m0, %0\n\ts_nop 0" ::"s"(v) : "memory");
}
template <int OFF> __device__ __forceinline__ void publish(uint32_t addr, uint32_t one) {
  asm volatile("s_mov_b64 exec, 1\n\tds_add_u32 %0, %1 offset:%2\n\ts_mov_b64 exec, -1" ::"v"(addr), "v"(one), "i"(OFF) : "memory");
}
__device__ __forceinline__ void await(volatile LDS uint32_t* p, uint32_t n) {
  while (__builtin_amdgcn_readfirstlane(*p) < n) __builtin_amdgcn_s_sleep(1);
  asm volatile("" ::: "memory");
}
#define FENCE() __builtin_amdgcn_sched_barrier(0)
#define PRIO(x) __builtin_amdgcn_s_setprio(x)

template <int N> struct IC { static constexpr int v = N; };
template <int B, int E, class F> __device__ __forceinline__ void sfor(F&& f) {
  if constexpr (B < E) { f(IC<B>{}); sfor<B + 1, E>(f); }
}

__global__ __launch_bounds__(512, 2) void s4d_fused_kernel(Params P
) {
  __shared__ __attribute__((aligned(256))) u8 smem_[SMEM];
  LDS u8* smem = (LDS u8*)smem_;
  const int tid = threadIdx.x, lane = tid & 63;
  const int w = __builtin_amdgcn_readfirstlane(tid >> 6);
  const int xcd = blockIdx.x & 7, wl = blockIdx.x >> 3;
  const int br = xcd >> 2;
  const int ch0 = (xcd & 3) * 144 + wl * 8;
  const int d = ch0 + w;
  STAMP(0);
  RSTAMP(14);
  uint32_t warm0, warm1, warm2;
  {
    uint64_t pc; uint32_t len;
    asm volatile("s_getpc_b64 %0\n.Ls4d_here:\n\ts_mov_b32 %1, .Ls4d_end-.Ls4d_here" : "=s"(pc), "=s"(len));
    const bool code = w < 4;
    const uint64_t base = code ? pc : (uint64_t)(uintptr_t)&lane_tab;
    const uint32_t last = code ? len - 4 : (uint32_t)sizeof(LaneTab) - 4;
    warm0 = *(const GLB uint32_t*)(base + min(64u * (tid & 255), last));
  }
  FENCE();
  typedef __attribute__((address_space(4))) const Params* KArg;
  const KArg ka = (KArg)__builtin_amdgcn_kernarg_segment_ptr();
  const uint16_t* pu  = ka->p[7 * br + 0];
  const uint16_t* pla = ka->p[7 * br + 1];
  const uint16_t* pai = ka->p[7 * br + 2];
  const uint16_t* pcr = ka->p[7 * br + 3];
  const uint16_t* pci = ka->p[7 * br + 4];
  const uint16_t* pdt = ka->p[7 * br + 5];
  const uint16_t* pds = ka->p[7 * br + 6];

  if (tid < 16) *(LDS uint32_t*)(smem + CNT + 4 * tid) = 0;
  GLB uint16_t* pout = (GLB uint16_t*)ka->out;
  asm volatile("" : "+s"(pout) : "s"(pla));
  FENCE();
  bar();
  PRIO(3);
  const uint32_t g_la = ((const uint32_t*)pla)[d * 64 + lane];
  const uint32_t g_ai = ((const uint32_t*)pai)[d * 64 + lane];
  const uint32_t g_cr = ((const uint32_t*)pcr)[d * 64 + lane];
  const uint32_t g_ci = ((const uint32_t*)pci)[d * 64 + lane];
  const uint32_t g_dt = pdt[d];
  const uint32_t g_ds = pds[d];
  const u32x4 lt = *(const u32x4*)lane_tab.v[5][lane];
  FENCE();
  {
    const u8* ux = (const u8*)pu + (xcd & 3) * 288 - (xcd & 1) * 32;
    auto uline = [&](uint32_t slot) {
      const uint32_t row = min(114u * wl + slot / 5u, (uint32_t)SEQ - 1);
      return *(const GLB uint32_t*)(ux + row * (DCH * 2) + (slot % 5u) * 64);
    };
    warm1 = uline(tid);
    warm2 = 0;
    if (w == 7) warm2 = uline(512 + min(lane, 57));
  }
  u32x4 b4e, b4o, b8e, b8o, cmask;
  u32x4 ur[4];
  const u8* up = (const u8*)pu + (size_t)(256 * w + 4 * lane) * (DCH * 2) + ch0 * 2;
  FENCE();

  LDS u8* reg = smem + YAREA + w * YREG;
  LDS u8* ub = smem + UOFF + w * USTRIDE;
  const int c16 = lane & 15, g4 = lane >> 4;
  const int r32 = lane & 31, h2 = lane >> 5;
  const uint32_t cnta = (uint32_t)(uintptr_t)(smem + CNT), one = 1;

  if (lane < 60) { u32x2 z = {0, 0}; *(LDS u32x2*)(ub + 8 * lane) = z; }

  LDS u8* xrd;
  LDS u8* ut = smem + UOFF + 2 * (UPAD + 256 * w + 4 * lane);
  LDS u8* yrd = reg + 272 * r32 + 16 * h2;
  const uint32_t m0x = (uint32_t)(uintptr_t)smem + w * XREG, m0y = (uint32_t)(uintptr_t)smem + w * YREG;

  const float dt = rbf(exp_acc(bflo(g_dt)));
  const float dskip = bflo(g_ds);
  STAMP(1);

  f32x16 kacc;
  #pragma unroll
  for (int i = 0; i < 16; i++) kacc[i] = 0.f;
  u32x4 fa[8], fb[8];
  sfor<0, 2>([&](auto RD) {
    constexpr int rd = RD.v;
    if constexpr (rd == 1) { PRIO(1); asm volatile("" ::"v"(warm0), "v"(warm1), "v"(warm2)); }
    const float la = rd ? bfhi(g_la) : bflo(g_la);
    const float ai = rd ? bfhi(g_ai) : bflo(g_ai);
    const float cr = rd ? bfhi(g_cr) : bflo(g_cr);
    const float ci = rd ? bfhi(g_ci) : bflo(g_ci);
    const float a = -rbf(exp_acc(la)) * dt, b = ai * dt;
    const float al = a * 1.44269504088896341f, be = b * 0.159154943091895336f;
    const f32x2 w1 = cpow_direct(al, be, 1.f);
    f32x2 y[4], t[5];
    y[1] = cpow_direct(al, be, 8.f); y[1].y = -y[1].y;
    y[0] = f32x2{1.f, 0.f};
    f32x2 x = {2.f * (cr * dt), 2.f * (ci * dt)};
    if constexpr (rd == 1) {
      FENCE();
      #pragma unroll
      for (int s = 0; s < 8; s++) kacc = mma32(fa[s], fb[s], kacc);
      asm volatile("" : "+v"(kacc));
    }
    t[0] = cm1(y[1], y[1]);
    y[2] = cm2(y[1], y[1], t[0]);
    t[0] = cm1(y[2], y[1]); t[1] = cm1(y[2], y[2]);
    y[3] = cm2(y[2], y[1], t[0]);
    const f32x2 y32 = cm2(y[2], y[2], t[1]);
    f32x2 xs[5];
    uint32_t xp[9];
    xs[0] = x;
    xp[0] = pk2(x);
    set_m0(m0y);
    sfor<0, 8>([&](auto I) {
      constexpr int i = I.v;
      if constexpr (i == 4) PRIO(2 - 2 * rd);
      uint32_t p[4];
      sfor<0, 4>([&](auto C) { p[C.v] = (i == 0 && C.v == 0) ? 0x00003F80u : pk2(y[C.v]); });
      t[4] = cm1(x, w1);
      sfor<0, 4>([&](auto C) {
        constexpr int c = C.v;
        if constexpr (i == 0 && c == 0) { }
        else if constexpr (i < 7 || c == 0) t[c] = cm1(y[c], y32);
        wr_tid<YAREA + 272 * (4 * i + c)>(p[c]);
      });
      x = cm2(x, w1, t[4]);
      sfor<0, 4>([&](auto C) {
        constexpr int c = C.v;
        if constexpr (i == 0 && c == 0) y[0] = y32;
        else if constexpr (i < 7 || c == 0) y[c] = cm2(y[c], y32, t[c]);
      });
      xp[i + 1] = pk2(x);
      if constexpr (rd == 0 && (i & 1) == 0) ur[i >> 1] = *(const u32x4*)(up + (i >> 1) * (DCH * 2));
      if constexpr (rd == 1 && i >= TSLOT && i < TSLOT + 4) {
        u32x2 lo, hi;
        lo.x = __builtin_amdgcn_perm(ur[1][i - TSLOT], ur[0][i - TSLOT], 0x05040100u);
        lo.y = __builtin_amdgcn_perm(ur[3][i - TSLOT], ur[2][i - TSLOT], 0x05040100u);
        hi.x = __builtin_amdgcn_perm(ur[1][i - TSLOT], ur[0][i - TSLOT], 0x07060302u);
        hi.y = __builtin_amdgcn_perm(ur[3][i - TSLOT], ur[2][i - TSLOT], 0x07060302u);
        *(LDS u32x2*)(ut + (2 * (i - TSLOT)) * USTRIDE) = lo;
        *(LDS u32x2*)(ut + (2 * (i - TSLOT) + 1) * USTRIDE) = hi;
        if constexpr (i == TSLOT + 3) publish<0>(cnta, one);
      }
      if constexpr (rd == 1 && i == 3) b4e = *(const u32x4*)lane_tab.v[0][lane];
      if constexpr (rd == 1 && i == 4) b4o = *(const u32x4*)lane_tab.v[1][lane];
      if constexpr (rd == 1 && i == 5) b8e = *(const u32x4*)lane_tab.v[2][lane];
      if constexpr (rd == 1 && i == 6) b8o = *(const u32x4*)lane_tab.v[3][lane];
      if constexpr (rd == 1 && i == 7) cmask = *(const u32x4*)lane_tab.v[4][lane];
      if constexpr (i & 1) xs[(i + 1) / 2] = x;
      FENCE();
    });
    if constexpr (rd == 1 && PTRANS) PRIO(PTRANS);
    const f32x2 v256 = y[0];
    set_m0(m0x);
    f32x2 xq[5], v512;
    sfor<0, 5>([&](auto J) { t[J.v] = cm1(xs[J.v], v256); wr_tid<16 * 17 * J.v>(xp[J.v]); });
    f32x2 tv = cm1(v256, v256);
    sfor<0, 4>([&](auto J) { wr_tid<16 * 17 * (5 + J.v)>(xp[5 + J.v]); });
    sfor<0, 5>([&](auto J) { xq[J.v] = cm2c(xs[J.v], v256, t[J.v]); });
    v512 = cm2(v256, v256, tv);
    t[0] = cm1(xq[2], v256); t[1] = cm1(xq[4], v256); t[2] = cm1(xq[2], v512); t[3] = cm1(xq[4], v512);
    wr_tid<16 * 186>(pk2(xq[0])); wr_tid<16 * 219>(pk2(xq[1])); wr_tid<16 * 153>(pk2(xq[2]));
    wr_tid<16 * 170>(pk2(xq[3])); wr_tid<16 * 203>(pk2(xq[4]));
    f32x2 x516 = cm2c(xq[2], v256, t[0]), x520 = cm2c(xq[4], v256, t[1]);
    f32x2 x772 = cm2c(xq[2], v512, t[2]), x776 = cm2c(xq[4], v512, t[3]);
    t[0] = cm1(x520, v512); t[1] = cm1(x776, v512);
    wr_tid<16 * 236>(pk2(x516)); wr_tid<16 * 269>(pk2(x520));
    f32x2 x1032 = cm2c(x520, v512, t[0]), x1288 = cm2c(x776, v512, t[1]);
    wr_tid<16 * 302>(pk2(x772)); wr_tid<16 * 335>(pk2(x776));
    t[0] = cm1(x1032, v512); t[1] = cm1(x1288, v512);
    wr_tid<16 * 252>(pk2(x1032)); wr_tid<16 * 285>(pk2(x1288));
    f32x2 x1544 = cm2c(x1032, v512, t[0]), x1800 = cm2c(x1288, v512, t[1]);
    wr_tid<16 * 318>(pk2(x1544)); wr_tid<16 * 351>(pk2(x1800));
    FENCE();
    if constexpr (rd == 0) xrd = smem + w * XREG + lt[0];
    #pragma unroll
    for (int s = 0; s < 8; s++) { fa[s] = ld128(xrd + 32 * s); fb[s] = ld128(yrd + 32 * s); }
    FENCE();
  });
  STAMP(2);
  {
    u32x2 z = {0, 0};
    asm volatile("" ::: "memory");
    *(LDS u32x2*)(reg + lt[1]) = z;
  }
  #pragma unroll
  for (int s = 0; s < 8; s++) kacc = mma32(fa[s], fb[s], kacc);

  {
    const int xo = 2 * (540 - 8 * r32 - 4 * h2);
    const float k0 = kacc[0] + ((lane == 0) ? dskip : 0.f);
    u32x2 v;
    v.x = pk(kacc[3], kacc[2]); v.y = pk(kacc[1], k0);            *(LDS u32x2*)(reg + KD1 + xo) = v;
    v.x = pk(kacc[7], kacc[6]); v.y = pk(kacc[5], kacc[4]);       *(LDS u32x2*)(reg + KD0 + xo) = v;
    v.x = pk(kacc[10], kacc[9]); v.y = pk(kacc[8], kacc[8]);      *(LDS u32x2*)(reg + KD1 + xo - 512) = v;
    v.x = pk(kacc[10], kacc[10]); v.y = pk(kacc[9], kacc[8]);     *(LDS u32x2*)(reg + KD0 + xo - 512) = v;
    if (lane == 63) {
      v.x = pk(0.f, kacc[10]); v.y = pk(kacc[10], kacc[10]);      *(LDS u32x2*)(reg + KD1 + 56) = v;
      v.x = 0; v.y = pk(kacc[10], kacc[10]);                      *(LDS u32x2*)(reg + KD0 + 56) = v;
    }
    if (lane == 0) { v.x = pk(k0, 0.f); v.y = 0;                  *(LDS u32x2*)(reg + KD0 + 1088) = v; }
    if (h2 == 0) {
      *(LDS uint32_t*)(reg + K4O + 2 * (190 - 2 * r32)) = pk(kacc[13], kacc[12]);
      *(LDS uint32_t*)(reg + K4O + 2 * (126 - 2 * r32)) = pk(kacc[15], kacc[14]);
    } else {
      uint32_t p0 = pk(kacc[12], kacc[13]), p1 = pk(kacc[14], kacc[15]);
      *(LDS uint16_t*)(reg + K8O + 2 * (127 - r32)) = (uint16_t)p0;
      *(LDS uint16_t*)(reg + K8O + 2 * (95 - r32)) = (uint16_t)(p0 >> 16);
      *(LDS uint16_t*)(reg + K8O + 2 * (63 - r32)) = (uint16_t)p1;
      *(LDS uint16_t*)(reg + K8O + 2 * (31 - r32)) = (uint16_t)(p1 >> 16);
    }
  }
  u32x4 kc;
  {
    uint32_t kv = __builtin_amdgcn_readlane(pk(kacc[15], kacc[15]), 31);
    #pragma unroll
    for (int i = 0; i < 4; i++) kc[i] = kv & cmask[i];
    asm volatile("" : "+v"(kc));
  }

  u32x4 kd[17], k4[6], k8[4];
  LDS u8* kb = reg + lt[2] - 64 * 16;
  LDS u8* kb2 = kb + 64 * 8;
  LDS u8* k4b = reg + K4O + 2 * (316 - 4 * c16 + 8 * g4) - 64 * 8;
  LDS u8* k4c = k4b + 8;
  LDS u8* k8b = reg + K8O + 2 * (254 - 2 * c16 + 8 * g4) - 64 * 7;
  asm volatile("" : "+v"(kb), "+v"(kb2), "+v"(k4b), "+v"(k4c), "+v"(k8b));
  auto ldkd = [&](auto Q) {
    constexpr int q = Q.v;
    LDS uint32_t* p = (LDS uint32_t*)(q > 8 ? kb + 64 * (16 - q) : kb2 + 64 * (8 - q));
    kd[q][0] = p[0]; kd[q][1] = p[1]; kd[q][2] = p[2]; kd[q][3] = p[3];
  };
  auto ldk4 = [&](auto I) {
    constexpr int s = I.v + 3;
    u32x2 lo = *(LDS u32x2*)(k4b + 64 * (8 - s)), hi = *(LDS u32x2*)(k4c + 64 * (8 - s));
    k4[s - 3][0] = lo.x; k4[s - 3][1] = lo.y; k4[s - 3][2] = hi.x; k4[s - 3][3] = hi.y;
  };
  auto ldk8 = [&](auto I) {
    constexpr int s = I.v + 4;
    LDS uint32_t* p = (LDS uint32_t*)(k8b + 64 * (7 - s));
    k8[s - 4][0] = p[0]; k8[s - 4][1] = p[1]; k8[s - 4][2] = p[2]; k8[s - 4][3] = p[3];
  };
  LDS u8* uf = ub + 2 * (UPAD - 16 + 16 * c16 + 8 * g4);
  LDS u8* u4 = ub + 2 * (UPAD - 24 + 8 * c16 + 8 * g4);
  LDS u8* ysw = reg + YSO + 64 * (w & 3) + 144 * g4 + 8 * c16;
  LDS u8* ytr = smem + lt[3] + 32 * (w & 3);
  GLB uint16_t* outp = pout + (size_t)(64 * (w & 3) + 16 * g4 + c16) * (2 * DCH) + br * DCH + ch0;
  const int grp = w >> 2;
  f32x4 acc[8];
  const f32x4 zero4 = {0.f, 0.f, 0.f, 0.f};
  #pragma unroll
  for (int i = 0; i < 8; i++) acc[i] = zero4;
  u32x4 df[64], f4[12], fe[4], fo[4], v8[4], v4a, v4b;
  f32x4 e4a, o4a, e4b, o4b, e8, o8;
  auto cvt8 = [&](f32x4 e, f32x4 o) { return u32x4{pk(e[0], o[0]), pk(e[1], o[1]), pk(e[2], o[2]), pk(e[3], o[3])}; };
  auto finish = [&](auto TT) {
    constexpr int T = TT.v;
    u32x2 y = {pk(acc[T][0], acc[T][1]), pk(acc[T][2], acc[T][3])};
    *(LDS u32x2*)(ysw + 576 * T) = y;
    publish<8 + 4 * T>(cnta, one);
  };
  uint32_t flag;
  s16x4 tlo, thi;
  auto out_poll = [&](int T) { flag = *(volatile LDS uint32_t*)(smem + CNT + 8 + 4 * T); };
  auto out_read = [&](int T) {
    if (__builtin_amdgcn_readfirstlane(flag) < 8) await((volatile LDS uint32_t*)(smem + CNT + 8 + 4 * T), 8);
    asm volatile("" ::: "memory");
    tlo = __builtin_amdgcn_ds_read_tr16_b64_v4i16((LDS s16x4*)(ytr + 576 * T));
    thi = __builtin_amdgcn_ds_read_tr16_b64_v4i16((LDS s16x4*)(ytr + 576 * T + 4 * YREG));
  };
  auto out_store = [&](int T) {
    u32x2 l2 = __builtin_bit_cast(u32x2, tlo), h2v = __builtin_bit_cast(u32x2, thi);
    u32x4 o4 = {l2.x, l2.y, h2v.x, h2v.y};
    *(GLB u32x4*)(outp + (size_t)(256 * T) * (2 * DCH)) = o4;
  };
  constexpr int PF = PFD;
  asm volatile("" ::"v"(b4e), "v"(b4o), "v"(b8e), "v"(b8o));
  flag = *(volatile LDS uint32_t*)(smem + CNT);
  sfor<0, 8>([&](auto I) { ldkd(IC<7 - I.v>{}); ldkd(IC<15 - I.v>{}); });
  ldkd(IC<16>{});
  FENCE();
  if (__builtin_amdgcn_readfirstlane(flag) < 8) await((volatile LDS uint32_t*)(smem + CNT), 8);
  PRIO(3);
  STAMP(8);
  #pragma unroll
  for (int n = 0; n < PF; n++) df[n] = ld128(uf + 64 * (n - 7));
  FENCE();
  sfor<0, 64>([&](auto NN) {
    constexpr int n = NN.v, S = n >> 3, k = n & 7, r = 7 - k;
    if constexpr (n == 24) PRIO(2);
    if constexpr (n == 40) PRIO(1);
    if constexpr (n == 56) PRIO(0);
    if constexpr (n + PF < 64) df[n + PF] = ld128(uf + 64 * (n + PF - 7));
    if constexpr (n >= 4 && n < 10) ldk4(IC<n - 4>{});
    if constexpr (n >= 18 && n < 22) ldk8(IC<n - 18>{});
    if constexpr (S >= 1 && S < 7 && (k == 4 || k == 5)) f4[2 * S - 6 + k] = ld128(u4 + 256 * (2 * S - 6 + k));
    if constexpr (S >= 3 && S < 7 && k == 6) fe[S - 3] = ld128(uf + 512 * (S - 3) - 16);
    if constexpr (S >= 3 && S < 7 && k == 7) fo[S - 3] = ld128(uf + 512 * (S - 3));
    if constexpr (S >= 2 && S < 6 && k == 4) { if (grp == 1) out_poll(S - 2); }
    if constexpr (S >= 2 && S < 6 && k == 5) { if (grp == 1) out_read(S - 2); }
    if constexpr (S >= 2 && S < 6 && k == 7) { if (grp == 1) out_store(S - 2); }
    if constexpr (S >= 2 && k == 0) { e4a = mma(f4[2 * S - 4], b4e, zero4); o4a = mma(f4[2 * S - 4], b4o, zero4); }
    if constexpr (S >= 2 && k == 1) { e4b = mma(f4[2 * S - 3], b4e, zero4); o4b = mma(f4[2 * S - 3], b4o, zero4); }
    if constexpr (S >= 4 && k == 2) {
      e8 = mma(fe[S - 4], b8e, zero4); o8 = mma(fo[S - 4], b8o, zero4);
      acc[S] = mma(kc, fo[S - 4], acc[S]);
    }
    acc[S] = mma(kd[r], df[n], acc[S]);
    if constexpr (S + 1 < 8) acc[S + 1] = mma(kd[8 + r], df[n], acc[S + 1]);
    if constexpr (r == 0 && S + 2 < 8) acc[S + 2] = mma(kd[16], df[n], acc[S + 2]);
    if constexpr (S >= 2 && (k == 3 || k == 4)) {
      constexpr int phi = 2 * S - 7 + k;
      sfor<0, 3>([&](auto TT) {
        constexpr int T = S + TT.v;
        if constexpr (T < 8) acc[T] = mma(k == 3 ? v4a : v4b, k4[2 * T - phi - 3], acc[T]);
      });
    }
    if constexpr (S >= 4 && k == 5)
      sfor<0, S - 4>([&](auto FF) { acc[S] = mma(v8[FF.v], k8[S - FF.v - 4], acc[S]); });
    if constexpr (S >= 4 && k == 6) acc[S] = mma(v8[S - 4], k8[0], acc[S]);
    if constexpr (S >= 1 && k == 1) finish(IC<S - 1>{});
    if constexpr (S >= 2 && k == 2) v4a = cvt8(e4a, o4a);
    if constexpr (S >= 2 && k == 3) v4b = cvt8(e4b, o4b);
    if constexpr (S >= 4 && k == 4) v8[S - 4] = cvt8(e8, o8);
    FENCE();
    if constexpr (n == 7) { STAMP(5); }
    if constexpr (n == 31) { STAMP(6); }
  });
  finish(IC<7>{});
  STAMP(10);
  PRIO(0);
  for (int T = grp ? 7 : 4; T < 7 + grp; T++) {
    out_poll(T);
    out_read(T);
    out_store(T);
  }
  asm volatile(".Ls4d_end:");
  STAMP(7);
  STAMP(9);
  RSTAMP(15);
}

extern "C" void kernel_launch(void* const* d_in, const int* in_sizes, int n_in,
                              void* d_out, int out_size, void* d_ws, size_t ws_size,
                              hipStream_t stream) {
  Params P;
  for (int i = 0; i < 14; i++) P.p[i] = (const uint16_t*)d_in[i];
  P.out = (uint16_t*)d_out;
  s4d_fused_kernel<<<144, 512, 0, stream>>>(P);
}
```

```cpp
#include <hip/hip_runtime.h>
#include <hip/hip_bf16.h>
#include <stdint.h>

typedef float f32x2 __attribute__((ext_vector_type(2)));
typedef float f32x4 __attribute__((ext_vector_type(4)));
typedef float f32x16 __attribute__((ext_vector_type(16)));
typedef __bf16 bf16x8 __attribute__((ext_vector_type(8)));
typedef __bf16 bf16x2 __attribute__((ext_vector_type(2)));
typedef uint32_t u32x4 __attribute__((ext_vector_type(4)));
typedef uint32_t u32x2 __attribute__((ext_vector_type(2)));
typedef short s16x4 __attribute__((ext_vector_type(4)));
typedef unsigned char u8;
#define LDS __attribute__((address_space(3)))
#define GLB __attribute__((address_space(1)))


constexpr int PP[9] = {3, 2, 1, 0, 2, 3, 2, 1, 0};
constexpr int ULD[4] = {2, 4, 6, 8}, TLD[5] = {11, 12, 13, 14, 15};
constexpr int STT[8] = {-1, -1, 0, 1, 2, -1, -1, 3}, STG[8] = {0, 0, 1, 1, 1, 0, 0, 0};
constexpr int STL = 4;
constexpr int PF = 3;

constexpr int SEQ = 2048, DCH = 576, NMODE = 128;
constexpr int UPAD = 240;
constexpr int USTRIDE = (UPAD + SEQ) * 2;
constexpr int UBYTES = 8 * USTRIDE;
constexpr int XREG = 5888, YREG = 8704;
constexpr int YAREA = 8 * XREG;
constexpr int UOFF = YAREA + 8 * YREG;
constexpr int CNT = UOFF + UBYTES;
constexpr int SMEM = CNT + 64;
constexpr int KD0 = 0, KD1 = 1408, K4O = 2528, K8O = 3040, YSO = 3392;

struct LaneTab { uint32_t v[6][64][4]; };
constexpr uint32_t band_el(int k, int half) { return (k >= -half && k <= half) ? 0x3F80u : 0u; }
constexpr LaneTab make_tab() {
  LaneTab t{};
  for (int lane = 0; lane < 64; lane++) {
    const int c16 = lane & 15, g4 = lane >> 4, r32 = lane & 31, h2 = lane >> 5;
    const int centre[4] = {8, 12, 8, 8}, half[4] = {2, 1, 4, 3};
    for (int m = 0; m < 4; m++)
      for (int i = 0; i < 4; i++) {
        const int k = 8 * g4 + 2 * i - centre[m] - c16;
        t.v[m][lane][i] = band_el(k, half[m]) | (band_el(k + 1, half[m]) << 16);
      }
    for (int i = 0; i < 4; i++) {
      const int k = 8 * g4 + 2 * i - c16;
      t.v[4][lane][i] = ((k == 12 || k == 13) ? 0xFFFFu : 0u) | ((k == 11 || k == 12) ? 0xFFFF0000u : 0u);
    }
    const int xt[8] = {186, 219, 153, 153, 153, 170, 203, 203};
    const int xunit = r32 < 8 ? 17 * r32 : r32 < 16 ? 17 * (r32 - 7) : r32 < 24 ? xt[r32 - 16]
                    : r32 < 28 ? 236 + 33 * (r32 - 24) : 252 + 33 * (r32 - 28);
    const int zoff = lane < 7 ? KD0 + 8 * lane : lane < 10 ? KD0 + 1096 + 8 * (lane - 7)
                   : lane < 17 ? KD1 + 8 * (lane - 10) : lane < 21 ? KD1 + 1088 + 8 * (lane - 17)
                   : lane < 37 ? K4O + 8 * (lane - 21) : lane < 53 ? K4O + 384 + 8 * (lane - 37)
                   : K8O + 256 + 8 * (lane - 53 < 7 ? lane - 53 : 7);
    t.v[5][lane][0] = 16 * xunit + 16 * h2;
    t.v[5][lane][1] = zoff;
    t.v[5][lane][2] = ((c16 & 1) ? KD1 : KD0) + 4 * (264 - ((c16 + 1) >> 1) + 4 * g4);
    t.v[5][lane][3] = YAREA + ((lane >> 2) & 3) * (YREG + 64) + YSO + 144 * (lane & 3) + 8 * g4;
  }
  return t;
}
__device__ const LaneTab lane_tab = make_tab();

struct Params { const uint16_t* p[14]; uint16_t* out; };

__device__ __forceinline__ float bflo(uint32_t v) { return __uint_as_float(v << 16); }
__device__ __forceinline__ float bfhi(uint32_t v) { return __uint_as_float(v & 0xFFFF0000u); }
__device__ __forceinline__ uint32_t pk(float lo, float hi) {
  f32x2 v = {lo, hi};
  return __builtin_bit_cast(uint32_t, __builtin_convertvector(v, bf16x2));
}
__device__ __forceinline__ uint32_t pk2(f32x2 v) { return __builtin_bit_cast(uint32_t, __builtin_convertvector(v, bf16x2)); }
__device__ __forceinline__ float rbf(float x) { return bflo(pk(x, x)); }
__device__ __forceinline__ float exp_acc(float x) {
  const float L = 1.44269502162933349609375f, Llo = 1.925963033500011e-8f;
  float t = x * L;
  float r = __builtin_fmaf(x, L, -t);
  r = __builtin_fmaf(x, Llo, r);
  float e = __builtin_amdgcn_exp2f(t);
  return __builtin_fmaf(e * r, 0.693147180559945f, e);
}
__device__ __forceinline__ f32x2 cmul(f32x2 x, f32x2 w) {
  f32x2 t = x * w.xx;
  f32x2 s = {-w.y, w.y};
  return __builtin_elementwise_fma(x.yx, s, t);
}
__device__ __forceinline__ f32x2 cm1(f32x2 x, f32x2 w) {
  f32x2 t;
  asm volatile("v_pk_mul_f32 %0, %1, %2 op_sel_hi:[1,0]" : "=v"(t) : "v"(x), "v"(w));
  return t;
}
__device__ __forceinline__ f32x2 cm2(f32x2 x, f32x2 w, f32x2 t) {
  f32x2 r;
  asm volatile("v_pk_fma_f32 %0, %1, %2, %3 op_sel:[1,1,0] op_sel_hi:[0,1,1] neg_lo:[0,1,0]" : "=v"(r) : "v"(x), "v"(w), "v"(t));
  return r;
}
__device__ __forceinline__ f32x2 cm2c(f32x2 x, f32x2 w, f32x2 t) {
  f32x2 r;
  asm volatile("v_pk_fma_f32 %0, %1, %2, %3 op_sel:[1,1,0] op_sel_hi:[0,1,1] neg_hi:[0,1,0]" : "=v"(r) : "v"(x), "v"(w), "v"(t));
  return r;
}
__device__ __forceinline__ f32x2 cpow_direct(float al, float be, float e) {
  float m = __builtin_amdgcn_exp2f(al * e);
  float ph = __builtin_amdgcn_fractf(be * e);
  f32x2 r = {m * __builtin_amdgcn_cosf(ph), m * __builtin_amdgcn_sinf(ph)};
  return r;
}
template <int OFF> __device__ __forceinline__ void wr_tid(uint32_t v) {
  asm volatile("ds_write_addtid_b32 %0 offset:%1" ::"v"(v), "i"(OFF) : "memory");
}
__device__ __forceinline__ f32x4 mma(u32x4 a, u32x4 b, f32x4 c) {
  return __builtin_amdgcn_mfma_f32_16x16x32_bf16(__builtin_bit_cast(bf16x8, a), __builtin_bit_cast(bf16x8, b), c, 0, 0, 0);
}
__device__ __forceinline__ f32x16 mma32(u32x4 a, u32x4 b, f32x16 c) {
  return __builtin_amdgcn_mfma_f32_32x32x16_bf16(__builtin_bit_cast(bf16x8, a), __builtin_bit_cast(bf16x8, b), c, 0, 0, 0);
}
__device__ __forceinline__ u32x4 ld128(LDS u8* p) { return *(LDS u32x4*)p; }
__device__ __forceinline__ void bar() {
  asm volatile("s_waitcnt lgkmcnt(0)\n\ts_barrier" ::: "memory");
}
__device__ __forceinline__ void set_m0(uint32_t v) {
  asm volatile("s_mov_b32 m0, %0\n\ts_nop 0" ::"s"(v) : "memory");
}
template <int OFF> __device__ __forceinline__ void publish(uint32_t addr, uint32_t one) {
  asm volatile("s_mov_b64 exec, 1\n\tds_add_u32 %0, %1 offset:%2\n\ts_mov_b64 exec, -1" ::"v"(addr), "v"(one), "i"(OFF) : "memory");
}
__device__ __forceinline__ void await(volatile LDS uint32_t* p, uint32_t n) {
  while (__builtin_amdgcn_readfirstlane(*p) < n) __builtin_amdgcn_s_sleep(1);
  asm volatile("" ::: "memory");
}
#define FENCE() __builtin_amdgcn_sched_barrier(0)
#define PRIO(x) __builtin_amdgcn_s_setprio(x)

template <int N> struct IC { static constexpr int v = N; };
template <int B, int E, class F> __device__ __forceinline__ void sfor(F&& f) {
  if constexpr (B < E) { f(IC<B>{}); sfor<B + 1, E>(f); }
}

__global__ __launch_bounds__(512, 2) void s4d_fused_kernel(Params P
) {
  __shared__ __attribute__((aligned(256))) u8 smem_[SMEM];
  LDS u8* smem = (LDS u8*)smem_;
  const int tid = threadIdx.x, lane = tid & 63;
  const int w = __builtin_amdgcn_readfirstlane(tid >> 6);
  const int xcd = blockIdx.x & 7, wl = blockIdx.x >> 3;
  const int br = xcd >> 2;
  const int ch0 = (xcd & 3) * 144 + wl * 8;
  const int d = ch0 + w;
  const int grp = w >> 2;
  uint32_t warm0, warm1, warm2;
  {
    uint64_t pc; uint32_t len;
    asm volatile("s_getpc_b64 %0\n.Ls4d_here:\n\ts_mov_b32 %1, .Ls4d_end-.Ls4d_here" : "=s"(pc), "=s"(len));
    const bool code = w < 4;
    const uint64_t base = code ? pc : (uint64_t)(uintptr_t)&lane_tab;
    const uint32_t last = code ? len - 4 : (uint32_t)sizeof(LaneTab) - 4;
    warm0 = *(const GLB uint32_t*)(base + min(64u * (tid & 255), last));
  }
  FENCE();
  typedef __attribute__((address_space(4))) const Params* KArg;
  const KArg ka = (KArg)__builtin_amdgcn_kernarg_segment_ptr();
  const uint16_t* pu  = ka->p[7 * br + 0];
  const uint16_t* pla = ka->p[7 * br + 1];
  const uint16_t* pai = ka->p[7 * br + 2];
  const uint16_t* pcr = ka->p[7 * br + 3];
  const uint16_t* pci = ka->p[7 * br + 4];
  const uint16_t* pdt = ka->p[7 * br + 5];
  const uint16_t* pds = ka->p[7 * br + 6];

  if (tid < 16) *(LDS uint32_t*)(smem + CNT + 4 * tid) = 0;
  GLB uint16_t* pout = (GLB uint16_t*)ka->out;
  asm volatile("" : "+s"(pout) : "s"(pla));
  FENCE();
  bar();
  PRIO(PP[0]);
  const uint32_t g_la = ((const uint32_t*)pla)[d * 64 + lane];
  const uint32_t g_ai = ((const uint32_t*)pai)[d * 64 + lane];
  const uint32_t g_cr = ((const uint32_t*)pcr)[d * 64 + lane];
  const uint32_t g_ci = ((const uint32_t*)pci)[d * 64 + lane];
  const uint32_t g_dt = pdt[d];
  const uint32_t g_ds = pds[d];
  const u32x4 lt = *(const u32x4*)lane_tab.v[5][lane];
  FENCE();
  {
    const u8* ux = (const u8*)pu + (xcd & 3) * 288 - (xcd & 1) * 32;
    auto uline = [&](uint32_t slot) {
      const uint32_t row = min(114u * wl + slot / 5u, (uint32_t)SEQ - 1);
      return *(const GLB uint32_t*)(ux + row * (DCH * 2) + (slot % 5u) * 64);
    };
    warm1 = uline(tid);
    warm2 = 0;
    if (w == 7) warm2 = uline(512 + min(lane, 57));
  }
  u32x4 b4e, b4o, b8e, b8o, cmask;
  u32x4 ur[4];
  const u8* up = (const u8*)pu + (size_t)(256 * w + 4 * lane) * (DCH * 2) + ch0 * 2;
  FENCE();

  LDS u8* reg = smem + YAREA + w * YREG;
  LDS u8* ub = smem + UOFF + w * USTRIDE;
  const int c16 = lane & 15, g4 = lane >> 4;
  const int r32 = lane & 31, h2 = lane >> 5;
  const uint32_t cnta = (uint32_t)(uintptr_t)(smem + CNT), one = 1;

  if (lane < 60) { u32x2 z = {0, 0}; *(LDS u32x2*)(ub + 8 * lane) = z; }

  LDS u8* xrd;
  LDS u8* ut = smem + UOFF + 2 * (UPAD + 256 * w + 4 * lane);
  LDS u8* yrd = reg + 272 * r32 + 16 * h2;
  const uint32_t m0x = (uint32_t)(uintptr_t)smem + w * XREG, m0y = (uint32_t)(uintptr_t)smem + w * YREG;

  const float dt = rbf(exp_acc(bflo(g_dt)));
  const float dskip = bflo(g_ds);

  f32x16 kacc;
  #pragma unroll
  for (int i = 0; i < 16; i++) kacc[i] = 0.f;
  u32x4 fa[8], fb[8];
  sfor<0, 2>([&](auto RD) {
    constexpr int rd = RD.v;
    if constexpr (rd == 1) { PRIO(PP[2]); }
    const float la = rd ? bfhi(g_la) : bflo(g_la);
    const float ai = rd ? bfhi(g_ai) : bflo(g_ai);
    const float cr = rd ? bfhi(g_cr) : bflo(g_cr);
    const float ci = rd ? bfhi(g_ci) : bflo(g_ci);
    const float a = -rbf(exp_acc(la)) * dt, b = ai * dt;
    const float al = a * 1.44269504088896341f, be = b * 0.159154943091895336f;
    const f32x2 w1 = cpow_direct(al, be, 1.f);
    f32x2 y[4], t[5];
    y[1] = cpow_direct(al, be, 8.f); y[1].y = -y[1].y;
    y[0] = f32x2{1.f, 0.f};
    f32x2 x = {2.f * (cr * dt), 2.f * (ci * dt)};
    if constexpr (rd == 1) {
      FENCE();
      #pragma unroll
      for (int s = 0; s < 8; s++) kacc = mma32(fa[s], fb[s], kacc);
      asm volatile("" : "+v"(kacc));
    }
    t[0] = cm1(y[1], y[1]);
    y[2] = cm2(y[1], y[1], t[0]);
    t[0] = cm1(y[2], y[1]); t[1] = cm1(y[2], y[2]);
    y[3] = cm2(y[2], y[1], t[0]);
    const f32x2 y32 = cm2(y[2], y[2], t[1]);
    f32x2 xs[5];
    uint32_t xp[9];
    xs[0] = x;
    xp[0] = pk2(x);
    set_m0(m0y);
    sfor<0, 8>([&](auto I) {
      constexpr int i = I.v;
      if constexpr (i == 4) PRIO(PP[rd ? 3 : 1]);
      uint32_t p[4];
      sfor<0, 4>([&](auto C) { p[C.v] = (i == 0 && C.v == 0) ? 0x00003F80u : pk2(y[C.v]); });
      t[4] = cm1(x, w1);
      sfor<0, 4>([&](auto C) {
        constexpr int c = C.v;
        if constexpr (i == 0 && c == 0) { }
        else if constexpr (i < 7 || c == 0) t[c] = cm1(y[c], y32);
        wr_tid<YAREA + 272 * (4 * i + c)>(p[c]);
      });
      x = cm2(x, w1, t[4]);
      sfor<0, 4>([&](auto C) {
        constexpr int c = C.v;
        if constexpr (i == 0 && c == 0) y[0] = y32;
        else if constexpr (i < 7 || c == 0) y[c] = cm2(y[c], y32, t[c]);
      });
      xp[i + 1] = pk2(x);
      sfor<0, 4>([&](auto J) { if constexpr (ULD[J.v] == 8 * rd + i) ur[J.v] = *(const u32x4*)(up + J.v * (DCH * 2)); });
      if constexpr (8 * rd + i == TLD[0]) b4e = *(const u32x4*)lane_tab.v[0][lane];
      if constexpr (8 * rd + i == TLD[1]) b4o = *(const u32x4*)lane_tab.v[1][lane];
      if constexpr (8 * rd + i == TLD[2]) b8e = *(const u32x4*)lane_tab.v[2][lane];
      if constexpr (8 * rd + i == TLD[3]) b8o = *(const u32x4*)lane_tab.v[3][lane];
      if constexpr (8 * rd + i == TLD[4]) cmask = *(const u32x4*)lane_tab.v[4][lane];
      if constexpr (i & 1) xs[(i + 1) / 2] = x;
      FENCE();
    });
    if constexpr (rd == 1) PRIO(PP[4]);
    const f32x2 v256 = y[0];
    set_m0(m0x);
    f32x2 xq[5], v512;
    sfor<0, 5>([&](auto J) { t[J.v] = cm1(xs[J.v], v256); wr_tid<16 * 17 * J.v>(xp[J.v]); });
    f32x2 tv = cm1(v256, v256);
    sfor<0, 4>([&](auto J) { wr_tid<16 * 17 * (5 + J.v)>(xp[5 + J.v]); });
    sfor<0, 5>([&](auto J) { xq[J.v] = cm2c(xs[J.v], v256, t[J.v]); });
    v512 = cm2(v256, v256, tv);
    t[0] = cm1(xq[2], v256); t[1] = cm1(xq[4], v256); t[2] = cm1(xq[2], v512); t[3] = cm1(xq[4], v512);
    wr_tid<16 * 186>(pk2(xq[0])); wr_tid<16 * 219>(pk2(xq[1])); wr_tid<16 * 153>(pk2(xq[2]));
    wr_tid<16 * 170>(pk2(xq[3])); wr_tid<16 * 203>(pk2(xq[4]));
    f32x2 x516 = cm2c(xq[2], v256, t[0]), x520 = cm2c(xq[4], v256, t[1]);
    f32x2 x772 = cm2c(xq[2], v512, t[2]), x776 = cm2c(xq[4], v512, t[3]);
    t[0] = cm1(x520, v512); t[1] = cm1(x776, v512);
    wr_tid<16 * 236>(pk2(x516)); wr_tid<16 * 269>(pk2(x520));
    f32x2 x1032 = cm2c(x520, v512, t[0]), x1288 = cm2c(x776, v512, t[1]);
    wr_tid<16 * 302>(pk2(x772)); wr_tid<16 * 335>(pk2(x776));
    t[0] = cm1(x1032, v512); t[1] = cm1(x1288, v512);
    wr_tid<16 * 252>(pk2(x1032)); wr_tid<16 * 285>(pk2(x1288));
    f32x2 x1544 = cm2c(x1032, v512, t[0]), x1800 = cm2c(x1288, v512, t[1]);
    wr_tid<16 * 318>(pk2(x1544)); wr_tid<16 * 351>(pk2(x1800));
    FENCE();
    if constexpr (rd == 0) xrd = smem + w * XREG + lt[0];
    #pragma unroll
    for (int s = 0; s < 8; s++) { fa[s] = ld128(xrd + 32 * s); fb[s] = ld128(yrd + 32 * s); }
    FENCE();
  });
  {
    u32x2 z = {0, 0};
    asm volatile("" ::: "memory");
    *(LDS u32x2*)(reg + lt[1]) = z;
  }
  asm volatile("" ::"v"(warm0), "v"(warm1), "v"(warm2));
  #pragma unroll
  for (int dd = 0; dd < 4; dd++) {
    u32x2 lo, hi;
    lo.x = __builtin_amdgcn_perm(ur[1][dd], ur[0][dd], 0x05040100u);
    lo.y = __builtin_amdgcn_perm(ur[3][dd], ur[2][dd], 0x05040100u);
    hi.x = __builtin_amdgcn_perm(ur[1][dd], ur[0][dd], 0x07060302u);
    hi.y = __builtin_amdgcn_perm(ur[3][dd], ur[2][dd], 0x07060302u);
    *(LDS u32x2*)(ut + (2 * dd) * USTRIDE) = lo;
    *(LDS u32x2*)(ut + (2 * dd + 1) * USTRIDE) = hi;
  }
  publish<0>(cnta + 4 * grp, one);
  #pragma unroll
  for (int s = 0; s < 8; s++) kacc = mma32(fa[s], fb[s], kacc);

  {
    const int xo = 2 * (540 - 8 * r32 - 4 * h2);
    const float k0 = kacc[0] + ((lane == 0) ? dskip : 0.f);
    u32x2 v;
    v.x = pk(kacc[3], kacc[2]); v.y = pk(kacc[1], k0);            *(LDS u32x2*)(reg + KD1 + xo) = v;
    v.x = pk(kacc[7], kacc[6]); v.y = pk(kacc[5], kacc[4]);       *(LDS u32x2*)(reg + KD0 + xo) = v;
    v.x = pk(kacc[10], kacc[9]); v.y = pk(kacc[8], kacc[8]);      *(LDS u32x2*)(reg + KD1 + xo - 512) = v;
    v.x = pk(kacc[10], kacc[10]); v.y = pk(kacc[9], kacc[8]);     *(LDS u32x2*)(reg + KD0 + xo - 512) = v;
    if (lane == 63) {
      v.x = pk(0.f, kacc[10]); v.y = pk(kacc[10], kacc[10]);      *(LDS u32x2*)(reg + KD1 + 56) = v;
      v.x = 0; v.y = pk(kacc[10], kacc[10]);                      *(LDS u32x2*)(reg + KD0 + 56) = v;
    }
    if (lane == 0) { v.x = pk(k0, 0.f); v.y = 0;                  *(LDS u32x2*)(reg + KD0 + 1088) = v; }
    if (h2 == 0) {
      *(LDS uint32_t*)(reg + K4O + 2 * (190 - 2 * r32)) = pk(kacc[13], kacc[12]);
      *(LDS uint32_t*)(reg + K4O + 2 * (126 - 2 * r32)) = pk(kacc[15], kacc[14]);
    } else {
      uint32_t p0 = pk(kacc[12], kacc[13]), p1 = pk(kacc[14], kacc[15]);
      *(LDS uint16_t*)(reg + K8O + 2 * (127 - r32)) = (uint16_t)p0;
      *(LDS uint16_t*)(reg + K8O + 2 * (95 - r32)) = (uint16_t)(p0 >> 16);
      *(LDS uint16_t*)(reg + K8O + 2 * (63 - r32)) = (uint16_t)p1;
      *(LDS uint16_t*)(reg + K8O + 2 * (31 - r32)) = (uint16_t)(p1 >> 16);
    }
  }
  u32x4 kc;
  const uint32_t kv = __builtin_amdgcn_readlane(pk(kacc[15], kacc[15]), 31);

  u32x4 kd[17], k4[6], k8[4];
  LDS u8* kb = reg + lt[2] - 64 * 16;
  LDS u8* kb2 = kb + 64 * 8;
  LDS u8* k4b = reg + K4O + 2 * (316 - 4 * c16 + 8 * g4) - 64 * 8;
  LDS u8* k4c = k4b + 8;
  LDS u8* k8b = reg + K8O + 2 * (254 - 2 * c16 + 8 * g4) - 64 * 7;
  asm volatile("" : "+v"(kb), "+v"(kb2), "+v"(k4b), "+v"(k4c), "+v"(k8b));
  auto ldkd = [&](auto Q) {
    constexpr int q = Q.v;
    LDS uint32_t* p = (LDS uint32_t*)(q > 8 ? kb + 64 * (16 - q) : kb2 + 64 * (8 - q));
    kd[q][0] = p[0]; kd[q][1] = p[1]; kd[q][2] = p[2]; kd[q][3] = p[3];
  };
  auto ldk4 = [&](auto I) {
    constexpr int s = I.v + 3;
    u32x2 lo = *(LDS u32x2*)(k4b + 64 * (8 - s)), hi = *(LDS u32x2*)(k4c + 64 * (8 - s));
    k4[s - 3][0] = lo.x; k4[s - 3][1] = lo.y; k4[s - 3][2] = hi.x; k4[s - 3][3] = hi.y;
  };
  auto ldk8 = [&](auto I) {
    constexpr int s = I.v + 4;
    LDS uint32_t* p = (LDS uint32_t*)(k8b + 64 * (7 - s));
    k8[s - 4][0] = p[0]; k8[s - 4][1] = p[1]; k8[s - 4][2] = p[2]; k8[s - 4][3] = p[3];
  };
  LDS u8* uf = ub + 2 * (UPAD - 16 + 16 * c16 + 8 * g4);
  LDS u8* u4 = ub + 2 * (UPAD - 24 + 8 * c16 + 8 * g4);
  LDS u8* ysw = reg + YSO + 64 * (w & 3) + 144 * g4 + 8 * c16;
  LDS u8* ytr = smem + lt[3] + 32 * (w & 3);
  GLB uint16_t* outp = pout + (size_t)(64 * (w & 3) + 16 * g4 + c16) * (2 * DCH) + br * DCH + ch0;
  f32x4 acc[8];
  const f32x4 zero4 = {0.f, 0.f, 0.f, 0.f};
  #pragma unroll
  for (int i = 0; i < 8; i++) acc[i] = zero4;
  u32x4 df[64], f4[12], fe[4], fo[4], v8[4], v4a, v4b;
  f32x4 e4a, o4a, e4b, o4b, e8, o8;
  auto cvt8 = [&](f32x4 e, f32x4 o) { return u32x4{pk(e[0], o[0]), pk(e[1], o[1]), pk(e[2], o[2]), pk(e[3], o[3])}; };
  auto finish = [&](auto TT) {
    constexpr int T = TT.v;
    u32x2 y = {pk(acc[T][0], acc[T][1]), pk(acc[T][2], acc[T][3])};
    *(LDS u32x2*)(ysw + 576 * T) = y;
    publish<8 + 4 * T>(cnta, one);
  };
  uint32_t flag, flagu;
  s16x4 tlo, thi;
  auto out_poll = [&](int T) { flag = *(volatile LDS uint32_t*)(smem + CNT + 8 + 4 * T); };
  auto out_read = [&](int T) {
    if (__builtin_amdgcn_readfirstlane(flag) < 8) await((volatile LDS uint32_t*)(smem + CNT + 8 + 4 * T), 8);
    asm volatile("" ::: "memory");
    tlo = __builtin_amdgcn_ds_read_tr16_b64_v4i16((LDS s16x4*)(ytr + 576 * T));
    thi = __builtin_amdgcn_ds_read_tr16_b64_v4i16((LDS s16x4*)(ytr + 576 * T + 4 * YREG));
  };
  auto out_store = [&](int T) {
    u32x2 l2 = __builtin_bit_cast(u32x2, tlo), h2v = __builtin_bit_cast(u32x2, thi);
    u32x4 o4 = {l2.x, l2.y, h2v.x, h2v.y};
    *(GLB u32x4*)(outp + (size_t)(256 * T) * (2 * DCH)) = o4;
  };
  flag = *(volatile LDS uint32_t*)(smem + CNT);
  sfor<0, 8>([&](auto I) { ldkd(IC<7 - I.v>{}); ldkd(IC<15 - I.v>{}); });
  ldkd(IC<16>{});
  FENCE();
  if (__builtin_amdgcn_readfirstlane(flag) < 4) await((volatile LDS uint32_t*)(smem + CNT), 4);
  PRIO(PP[5]);
  #pragma unroll
  for (int n = 0; n < PF; n++) df[n] = ld128(uf + 64 * (n - 7));
  FENCE();
  sfor<0, 64>([&](auto NN) {
    constexpr int n = NN.v, S = n >> 3, k = n & 7, r = 7 - k;
    constexpr int ST_T = STT[S], ST_G = STG[S];
    if constexpr (n == 24) PRIO(PP[6]);
    if constexpr (n == 40) PRIO(PP[7]);
    if constexpr (n == 56) PRIO(PP[8]);
    if constexpr (n == 12) {
      #pragma unroll
      for (int i = 0; i < 4; i++) kc[i] = kv & cmask[i];
      asm volatile("" : "+v"(kc) : "v"(b4e), "v"(b4o), "v"(b8e), "v"(b8o));
    }
    if constexpr (n == 23) flagu = *(volatile LDS uint32_t*)(smem + CNT + 4);
    if constexpr (n == 26) { if (__builtin_amdgcn_readfirstlane(flagu) < 4) await((volatile LDS uint32_t*)(smem + CNT + 4), 4); }
    if constexpr (n + PF < 64) df[n + PF] = ld128(uf + 64 * (n + PF - 7));
    if constexpr (n >= 4 && n < 10) ldk4(IC<n - 4>{});
    if constexpr (n >= 18 && n < 22) ldk8(IC<n - 18>{});
    if constexpr (S >= 1 && S < 7 && (k == 4 || k == 5)) f4[2 * S - 6 + k] = ld128(u4 + 256 * (2 * S - 6 + k));
    if constexpr (S >= 3 && S < 7 && k == 6) fe[S - 3] = ld128(uf + 512 * (S - 3) - 16);
    if constexpr (S >= 3 && S < 7 && k == 7) fo[S - 3] = ld128(uf + 512 * (S - 3));
    if constexpr (ST_T >= 0 && k == 4) { if (grp == ST_G) out_poll(ST_T); }
    if constexpr (ST_T >= 0 && k == 5) { if (grp == ST_G) out_read(ST_T); }
    if constexpr (ST_T >= 0 && k == 7) { if (grp == ST_G) out_store(ST_T); }
    if constexpr (S >= 2 && k == 0) { e4a = mma(f4[2 * S - 4], b4e, zero4); o4a = mma(f4[2 * S - 4], b4o, zero4); }
    if constexpr (S >= 2 && k == 1) { e4b = mma(f4[2 * S - 3], b4e, zero4); o4b = mma(f4[2 * S - 3], b4o, zero4); }
    if constexpr (S >= 4 && k == 2) {
      e8 = mma(fe[S - 4], b8e, zero4); o8 = mma(fo[S - 4], b8o, zero4);
      acc[S] = mma(kc, fo[S - 4], acc[S]);
    }
    acc[S] = mma(kd[r], df[n], acc[S]);
    if constexpr (S + 1 < 8) acc[S + 1] = mma(kd[8 + r], df[n], acc[S + 1]);
    if constexpr (r == 0 && S + 2 < 8) acc[S + 2] = mma(kd[16], df[n], acc[S + 2]);
    if constexpr (S >= 2 && (k == 3 || k == 4)) {
      constexpr int phi = 2 * S - 7 + k;
      sfor<0, 3>([&](auto TT) {
        constexpr int T = S + TT.v;
        if constexpr (T < 8) acc[T] = mma(k == 3 ? v4a : v4b, k4[2 * T - phi - 3], acc[T]);
      });
    }
    if constexpr (S >= 4 && k == 5)
      sfor<0, S - 4>([&](auto FF) { acc[S] = mma(v8[FF.v], k8[S - FF.v - 4], acc[S]); });
    if constexpr (S >= 4 && k == 6) acc[S] = mma(v8[S - 4], k8[0], acc[S]);
    if constexpr (S >= 1 && k == 1) finish(IC<S - 1>{});
    if constexpr (S >= 2 && k == 2) v4a = cvt8(e4a, o4a);
    if constexpr (S >= 2 && k == 3) v4b = cvt8(e4b, o4b);
    if constexpr (S >= 4 && k == 4) v8[S - 4] = cvt8(e8, o8);
    {
      constexpr int nm = 1 + (S + 1 < 8) + (r == 0 && S + 2 < 8) + (S >= 2 && k < 2 ? 2 : 0) + (S >= 4 && k == 2 ? 3 : 0)
                       + (S >= 2 && (k == 3 || k == 4) ? (8 - S < 3 ? 8 - S : 3) : 0) + (S >= 4 && k == 5 ? S - 4 : 0) + (S >= 4 && k == 6);
      sfor<0, nm>([&](auto) {
        __builtin_amdgcn_sched_group_barrier(0x008, 1, 0);
        __builtin_amdgcn_sched_group_barrier(0x082, 2, 0);
      });
    }
    FENCE();
  });
  finish(IC<7>{});
  PRIO(0);
  for (int T = grp ? 7 : STL; T < 7 + grp; T++) {
    out_poll(T);
    out_read(T);
    out_store(T);
  }
  asm volatile(".Ls4d_end:");
}

extern "C" void kernel_launch(void* const* d_in, const int* in_sizes, int n_in,
                              void* d_out, int out_size, void* d_ws, size_t ws_size,
                              hipStream_t stream) {
  Params P;
  for (int i = 0; i < 14; i++) P.p[i] = (const uint16_t*)d_in[i];
  P.out = (uint16_t*)d_out;
  s4d_fused_kernel<<<144, 512, 0, stream>>>(P);
}
```
